```python
import torch

def _softmax(x, axis=-1):
    return torch.softmax(x, dim=axis)
def _gelu(x):
    return torch.nn.functional.gelu(x, approximate="tanh")
def _silu(x):
    return torch.nn.functional.silu(x)
def _sigmoid(x):
    return torch.sigmoid(x)
def _layernorm(x, axis=-1, eps=1e-5):
    m = x.mean(dim=axis, keepdim=True)
    v = x.var(dim=axis, keepdim=True, unbiased=False)
    return (x - m) / torch.sqrt(v + eps)
def _scatter_add(tgt, idx, src, axis=0):
    return tgt.clone().index_add_(axis, idx, src)
def _segment_sum(src, seg, n):
    out = torch.zeros((n,) + src.shape[1:], dtype=src.dtype)
    return out.index_add_(0, seg, src)
def _pad(x, pw):
    flat = tuple(v for pair in reversed(pw) for v in pair)
    return torch.nn.functional.pad(x, flat)

kernel_name = 'recurrent_scan__gqa_block_cached__glu_gated_geglu__decode'

def setup_inputs():

    _w = (lambda a: (lambda t: t.to(torch.bfloat16) if t.is_floating_point() else t)(torch.as_tensor(a)))
    def _A_setup():
        B, Sq, D = 1, 1, 1024
        Hq, Hkv, Sk, Dh = 32, 4, 65536, 128
        assert Hq % Hkv == 0, (Hq, Hkv)
        return {
            "x": _w((lambda *s: torch.randn(*s, dtype=torch.bfloat16) * 0.4)(B, Sq, D)),
            "k_cache": _w((lambda *s: torch.randn(*s, dtype=torch.bfloat16) * 1.6)(B, Hkv, Sk, Dh)),
            "v_cache": _w((lambda *s: torch.randn(*s, dtype=torch.bfloat16) * 1.6)(B, Hkv, Sk, Dh)),
            "wq": _w((lambda *s: torch.randn(*s, dtype=torch.bfloat16) * 1.6)(D, Hq * Dh) * (D ** -0.5)),
            "wo": _w((lambda *s: torch.randn(*s, dtype=torch.bfloat16) * 1.6)(Hq * Dh, D) * ((Hq * Dh) ** -0.5)),
        }
    _A_in = _A_setup()

    def _B_setup():
        B, S, Dm, Df = 1, 1, 1024, 3584
        return {
            "x": _w((lambda *s: torch.randn(*s, dtype=torch.bfloat16) * 0.4)(B, S, Dm)),
            "w_gate": _w((lambda *s: torch.randn(*s, dtype=torch.bfloat16) * 1.6)(Dm, Df) * (Dm ** -0.5)),
            "w_up": _w((lambda *s: torch.randn(*s, dtype=torch.bfloat16) * 1.6)(Dm, Df) * (Dm ** -0.5)),
            "w_down": _w((lambda *s: torch.randn(*s, dtype=torch.bfloat16) * 1.6)(Df, Dm) * (Df ** -0.5)),
        }
    _B_in = _B_setup()

    return {
        "A_x": _A_in["x"],
        "A_k_cache": _A_in["k_cache"],
        "A_v_cache": _A_in["v_cache"],
        "A_wq": _A_in["wq"],
        "A_wo": _A_in["wo"],
        "B_x": _B_in["x"],
        "B_w_gate": _B_in["w_gate"],
        "B_w_up": _B_in["w_up"],
        "B_w_down": _B_in["w_down"],
    }

def reference(A_x, A_k_cache, A_v_cache, A_wq, A_wo, B_x, B_w_gate, B_w_up, B_w_down):
    def _A_ref(x, k_cache, v_cache, wq, wo):
        B, Sq, D = x.shape
        Hq, Dh = 32, 128
        Hkv = k_cache.shape[1]
        rep = Hq // Hkv
        q = (lambda x, *d: x.permute(*d))((x @ wq).reshape(B, Sq, Hq, Dh), 0, 2, 1, 3)
        k = (lambda x, n, axis: torch.repeat_interleave(x, n, dim=axis))(k_cache, rep, axis=1)
        v = (lambda x, n, axis: torch.repeat_interleave(x, n, dim=axis))(v_cache, rep, axis=1)
        scores = torch.einsum("bhqd,bhkd->bhqk", q, k) * (Dh ** -0.5)
        p = _softmax(scores, axis=-1)
        o = torch.einsum("bhqk,bhkd->bhqd", p, v)
        return (lambda x, *d: x.permute(*d))(o, 0, 2, 1, 3).reshape(B, Sq, Hq * Dh) @ wo

    def _B_ref(x, w_gate, w_up, w_down):
        g = torch.einsum("bsd,df->bsf", x, w_gate)
        u = torch.einsum("bsd,df->bsf", x, w_up)
        h = _gelu(g) * u
        return torch.einsum("bsf,fd->bsd", h, w_down)

    x = A_x
    xn = _layernorm(x, axis=-1)
    s = _A_ref(xn, A_k_cache, A_v_cache, A_wq, A_wo)
    g = _silu(_B_ref(xn, B_w_gate, B_w_up, B_w_down))
    return x + s * g
```

```cpp
#include <hip/hip_runtime.h>
#include <hip/hip_bf16.h>
#include <stdint.h>

#define DIAG 0

namespace {
#define GAS __attribute__((address_space(1)))
#define LAS __attribute__((address_space(3)))
typedef unsigned v4u __attribute__((ext_vector_type(4)));
typedef unsigned v2u __attribute__((ext_vector_type(2)));
typedef float f32x4 __attribute__((ext_vector_type(4)));
typedef short bf16x8 __attribute__((ext_vector_type(8)));
typedef short bf16x4 __attribute__((ext_vector_type(4)));
typedef __amdgpu_buffer_rsrc_t rsrc_t;
typedef GAS unsigned long long gu64;

constexpr int DM = 1024, SK = 65536, DF = 3584;
constexpr int NCU = 256, NW = 8, NTHR = NW * 64;
constexpr int AUX_NT = 2, AUX_SC1 = 16;
constexpr int RSRC_W3 = 0x00020000;
constexpr unsigned SENT = 0xFFFFFFFFu;
constexpr unsigned SPIN_CAP = 1u << 16;

constexpr unsigned WS_EPOCH = 0;
constexpr unsigned WS_FLAG = 256;
constexpr unsigned WS_QG = WS_FLAG + 2048;
constexpr unsigned WS_QP = WS_QG + 32768;
constexpr unsigned WS_OUT = WS_QP + 256 * 1024;
constexpr unsigned OB_F1 = 0, OB_F3 = 1792, OB_A1 = OB_F3 + 4096, OB_A3 = OB_A1 + 5120, OB_BYTES = OB_A3 + 4096, OB_STRIDE = 15232;
constexpr unsigned WS_END = WS_OUT + NCU * OB_STRIDE;
constexpr unsigned WS_DIAG = 8u << 20;
static_assert(WS_OUT % 128 == 0 && OB_BYTES <= OB_STRIDE && OB_STRIDE % 128 == 0 && WS_END < WS_DIAG, "ws map");

constexpr unsigned L_RED = 0;
constexpr unsigned L_Q = 2048;
constexpr unsigned L_F1 = L_Q + 2048;
constexpr unsigned L_O = L_F1 + 512;
constexpr unsigned L_ML = L_O + 128;
constexpr unsigned L_GU = L_ML + 512;
constexpr unsigned L_MRG = L_GU + 8 * 448 * 4;
constexpr unsigned L_END = L_MRG + 32768;
constexpr unsigned LDS_BYTES = 96 * 1024;
static_assert(L_END <= LDS_BYTES && L_GU % 16 == 0 && L_MRG % 16 == 0, "lds map");

__device__ __forceinline__ float bf2f(unsigned h) { return __builtin_bit_cast(float, h << 16); }
__device__ __forceinline__ float bflo(unsigned u) { return __builtin_bit_cast(float, u << 16); }
__device__ __forceinline__ float bfhi(unsigned u) { return __builtin_bit_cast(float, u & 0xffff0000u); }
__device__ __forceinline__ unsigned f2bf(float f) { return (unsigned)__builtin_bit_cast(unsigned short, (__bf16)f); }
__device__ __forceinline__ float rbf(float f) { return bf2f(f2bf(f)); }
__device__ __forceinline__ unsigned pk2(float lo, float hi) {
    typedef __bf16 b2 __attribute__((ext_vector_type(2))); typedef float f2 __attribute__((ext_vector_type(2)));
    return __builtin_bit_cast(unsigned, __builtin_convertvector((f2){lo, hi}, b2)); }
__device__ __forceinline__ unsigned fbits(float f) { return __builtin_bit_cast(unsigned, f); }
__device__ __forceinline__ float bitsf(unsigned u) { return __builtin_bit_cast(float, u); }
__device__ __forceinline__ unsigned nosent(float f) { const unsigned u = fbits(f); return u < 0xFFFFFFFEu ? u : 0xFFFFFFFEu; }
template <int CTRL> __device__ __forceinline__ float dpp(float x) { return bitsf((unsigned)__builtin_amdgcn_mov_dpp((int)fbits(x), CTRL, 0xf, 0xf, true)); }
constexpr int ROR1 = 0x121, ROR2 = 0x122, ROR4 = 0x124, ROR8 = 0x128;
__device__ __forceinline__ float xrow_sum(float x) {
    auto s = __builtin_amdgcn_permlane16_swap(fbits(x), fbits(x), false, false); x = bitsf(s[0]) + bitsf(s[1]);
    auto t = __builtin_amdgcn_permlane32_swap(fbits(x), fbits(x), false, false); return bitsf(t[0]) + bitsf(t[1]); }
__device__ __forceinline__ float xrow_max(float x) {
    auto s = __builtin_amdgcn_permlane16_swap(fbits(x), fbits(x), false, false); x = fmaxf(bitsf(s[0]), bitsf(s[1]));
    auto t = __builtin_amdgcn_permlane32_swap(fbits(x), fbits(x), false, false); return fmaxf(bitsf(t[0]), bitsf(t[1])); }
__device__ __forceinline__ float wave_sum(float x) { x += dpp<ROR1>(x); x += dpp<ROR2>(x); x += dpp<ROR4>(x); x += dpp<ROR8>(x); return xrow_sum(x); }
__device__ __forceinline__ float wave_max(float x) { x = fmaxf(x, dpp<ROR1>(x)); x = fmaxf(x, dpp<ROR2>(x)); x = fmaxf(x, dpp<ROR4>(x)); x = fmaxf(x, dpp<ROR8>(x)); return xrow_max(x); }
__device__ __forceinline__ rsrc_t mk_rsrc(const void* p, unsigned bytes) { return __builtin_amdgcn_make_buffer_rsrc((void*)p, 0, (int)bytes, RSRC_W3); }
__device__ __forceinline__ v4u ld_nt(rsrc_t r, int voff, int soff) { return __builtin_amdgcn_raw_buffer_load_b128(r, voff, soff, AUX_NT); }
__device__ __forceinline__ v4u ld_sc1(rsrc_t r, int voff, int soff) { return __builtin_amdgcn_raw_buffer_load_b128(r, voff, soff, AUX_SC1); }
__device__ __forceinline__ v2u ld_sc1_64(rsrc_t r, int voff, int soff) { return __builtin_amdgcn_raw_buffer_load_b64(r, voff, soff, AUX_SC1); }
__device__ __forceinline__ void st_sc1(v4u v, rsrc_t r, int voff, int base) { __builtin_amdgcn_raw_buffer_store_b128(v, r, voff + base, 0, AUX_SC1); }
__device__ __forceinline__ unsigned umax4(v4u v) { const unsigned a = v.x > v.y ? v.x : v.y, b = v.z > v.w ? v.z : v.w; return a > b ? a : b; }
__device__ __forceinline__ unsigned umax2(unsigned a, unsigned b) { return a > b ? a : b; }
#define PIN8(a) asm volatile("" : "+v"(a[0]), "+v"(a[1]), "+v"(a[2]), "+v"(a[3]), "+v"(a[4]), "+v"(a[5]), "+v"(a[6]), "+v"(a[7]))
__device__ __forceinline__ f32x4 mfma32(v4u a, v4u b, f32x4 c) { return __builtin_amdgcn_mfma_f32_16x16x32_bf16(__builtin_bit_cast(bf16x8, a), __builtin_bit_cast(bf16x8, b), c, 0, 0, 0); }
__device__ __forceinline__ f32x4 mfma16(v2u a, v2u b, f32x4 c) { return __builtin_amdgcn_mfma_f32_16x16x16bf16_1k(__builtin_bit_cast(bf16x4, a), __builtin_bit_cast(bf16x4, b), c, 0, 0, 0); }
#define SB() __builtin_amdgcn_sched_barrier(0)
#define REPOLL() asm volatile("" ::: "memory")
#define WG_BARRIER() asm volatile("s_waitcnt lgkmcnt(0)\n\ts_barrier" ::: "memory")
constexpr int OOB = (int)0x80000000u;
constexpr float LOG2E = 1.4426950408889634f;

#define QP_AT 6
#define QG_AT 16
#define FLG_AT 2
#define RETRY(k) do { } while (0)
#define STAMP(k) do { } while (0)

struct Params {
    const void* x; const void* kc; const void* vc; const void* wq; const void* wo; const void* wg; const void* wu; const void* wd;
    void* out; void* ws;
};

__device__ __forceinline__ v4u gemv16(const v4u (&wv)[4], v2u a, int lane) {
    f32x4 acc[8];
#pragma unroll
    for (int d = 0; d < 4; ++d) {
        v2u lo, hi;
        lo.x = __builtin_amdgcn_perm(wv[1][d], wv[0][d], 0x05040100u); lo.y = __builtin_amdgcn_perm(wv[3][d], wv[2][d], 0x05040100u);
        hi.x = __builtin_amdgcn_perm(wv[1][d], wv[0][d], 0x07060302u); hi.y = __builtin_amdgcn_perm(wv[3][d], wv[2][d], 0x07060302u);
        acc[2 * d] = mfma16(a, lo, f32x4{0.f, 0.f, 0.f, 0.f});
        acc[2 * d + 1] = mfma16(a, hi, f32x4{0.f, 0.f, 0.f, 0.f});
    }
    const bool up = (lane >> 4) & 1;
    v4u o;
    o.x = nosent(up ? acc[4][0] : acc[0][0]); o.y = nosent(up ? acc[5][0] : acc[1][0]);
    o.z = nosent(up ? acc[6][0] : acc[2][0]); o.w = nosent(up ? acc[7][0] : acc[3][0]);
    return o;
}

__global__ void __launch_bounds__(NTHR, 2) fused_decode(Params p) {
    extern __shared__ __attribute__((aligned(16))) unsigned char smem[];
    LAS unsigned char* L = (LAS unsigned char*)smem;
    const int tid = threadIdx.x, lane = tid & 63, w = __builtin_amdgcn_readfirstlane(tid >> 6);
    const int b = blockIdx.x, xl = b & 7, ii = b >> 3, g = ii >> 3, r = 8 * xl + (ii & 7), cid = 64 * g + r;
    const int gg = lane >> 4, c16 = lane & 15;
    STAMP(0);
    const rsrc_t rX = mk_rsrc(p.x, DM * 2), rK = mk_rsrc(p.kc, 4u * SK * 256), rV = mk_rsrc(p.vc, 4u * SK * 256);
    const rsrc_t rWq = mk_rsrc(p.wq, DM * 4096 * 2), rWo = mk_rsrc(p.wo, 4096 * DM * 2), rWd = mk_rsrc(p.wd, DF * DM * 2);
    const rsrc_t rWS = mk_rsrc(p.ws, WS_END);
    const int ks = cid >> 4, cg = cid & 15;
    const rsrc_t rGU = mk_rsrc((cg >> 3) ? p.wu : p.wg, DM * DF * 2);
    const int obase = WS_OUT + cid * OB_STRIDE;

    const unsigned long long E = __hip_atomic_load((gu64*)p.ws, __ATOMIC_RELAXED, __HIP_MEMORY_SCOPE_AGENT);
    const int tj = ii & 3;
    const v4u xa = __builtin_amdgcn_raw_buffer_load_b128(rX, lane * 32, 0, 0), xb = __builtin_amdgcn_raw_buffer_load_b128(rX, lane * 32 + 16, 0, 0);
    unsigned xq[4];
#pragma unroll
    for (int t = 0; t < 4; ++t) xq[t] = __builtin_amdgcn_raw_buffer_load_b16(rX, (256 * tj + 64 * t + 8 * w + (lane >> 3)) * 2, 0, 0);
    const unsigned xg = __builtin_amdgcn_raw_buffer_load_b16(rX, (64 * ks + 8 * w + (lane & 7)) * 2, 0, 0);
    SB();
    v4u wq[4];
    {
        const int vo = (256 * tj + 8 * w + (lane >> 3)) * 8192 + (16 * (cid - tj)) * 2 + (lane & 7) * 16;
#pragma unroll
        for (int t = 0; t < 4; ++t) wq[t] = ld_nt(rWq, vo, t * 64 * 8192);
    }
    SB();
    {
        const v4u s4 = {SENT, SENT, SENT, SENT};
        st_sc1(s4, rWS, tid * 16, obase);
        st_sc1(s4, rWS, tid < (int)(OB_BYTES / 16 - NTHR) ? (NTHR + tid) * 16 : OOB, obase);
    }
    SB();
    const unsigned long long tag48 = (E + 1) & 0xFFFFFFFFFFFFull;
    const unsigned th = (unsigned)(tag48 >> 32), tl = (unsigned)tag48;
    auto badtag = [&](v4u q) { return (((q.x ^ tl) | (q.z ^ tl)) | (((q.y ^ th) | (q.w ^ th)) & 0xffffu)) != 0u; };

    float mean_b, sd_b;
    {
        float xv[16];
#pragma unroll
        for (int d = 0; d < 4; ++d) { xv[2 * d] = bflo(xa[d]); xv[2 * d + 1] = bfhi(xa[d]); xv[8 + 2 * d] = bflo(xb[d]); xv[8 + 2 * d + 1] = bfhi(xb[d]); }
        float s = 0.f;
#pragma unroll
        for (int d = 0; d < 16; ++d) s += xv[d];
        const float mean = wave_sum(s) * (1.0f / DM);
        float v = 0.f;
#pragma unroll
        for (int d = 0; d < 16; ++d) { const float t = xv[d] - mean; v = fmaf(t, t, v); }
        const float var = rbf(wave_sum(v) * (1.0f / DM));
        mean_b = rbf(mean);
        sd_b = rbf(sqrtf(rbf(var + 1e-5f)));
        asm volatile("" : "+v"(mean_b), "+v"(sd_b));
    }
    auto norm = [&](unsigned xbits) -> float { return rbf(rbf(bf2f(xbits) - mean_b) / sd_b); };
    STAMP(1);
    SB();
    v4u gu[8];
    {
        const int vo = lane < 56 ? (448 * (cg & 7) + 8 * lane) * 2 : OOB;
        const int so = (64 * ks + 8 * w) * (DF * 2);
#pragma unroll
        for (int j = 0; j < 8; ++j) gu[j] = ld_nt(rGU, vo, so + j * (DF * 2));
    }
    SB();

    {
        float acc[8];
#pragma unroll
        for (int e = 0; e < 8; ++e) acc[e] = 0.f;
#pragma unroll
        for (int t = 0; t < 4; ++t) { const float xn = norm(xq[t]);
#pragma unroll
            for (int d = 0; d < 4; ++d) { acc[2 * d] = fmaf(xn, bflo(wq[t][d]), acc[2 * d]); acc[2 * d + 1] = fmaf(xn, bfhi(wq[t][d]), acc[2 * d + 1]); } }
#pragma unroll
        for (int e = 0; e < 8; ++e) { float v = acc[e]; v += dpp<ROR8>(v); acc[e] = xrow_sum(v); }
        if (lane < 8) {
            LAS f32x4* red = (LAS f32x4*)(L + L_RED + w * 256 + lane * 32);
            red[0] = f32x4{acc[0], acc[1], acc[2], acc[3]}; red[1] = f32x4{acc[4], acc[5], acc[6], acc[7]};
        }
    }
    WG_BARRIER();
    if (w == 0) {
        float q = 0.f;
#pragma unroll
        for (int ww = 0; ww < 8; ++ww) q += ((LAS float*)(L + L_RED))[ww * 64 + lane];
        const unsigned u = fbits(q);
        st_sc1(v4u{tl, th | (u & 0xffff0000u), tl, th | (u << 16)}, rWS, lane * 16, WS_QP + cid * 1024);
    }
    STAMP(2);
    SB();

    const int kvo = (4 * (lane & 3) + ((lane >> 2) & 1)) * 256 + ((lane >> 3) & 1) * 64 + gg * 16;
    const int tbase = g * (SK * 256) + (8 * r + w) * 8192;
    auto kload = [&](int j) -> v4u { const int t = j >> 3, U = (j >> 1) & 3, s = j & 1; return ld_nt(rK, kvo, tbase + t * (512 * 8192) + (16 * (U & 1) + 2 * (U >> 1)) * 256 + s * 128); };
    auto vload = [&](int j) -> v4u { const int t = j >> 3, jj = j & 7; return ld_nt(rV, lane * 16, tbase + t * (512 * 8192) + jj * 1024); };
    v4u kb[32];
    v4u qp = {0u, 0u, 0u, 0u};
    const int qpo = WS_QP + (cid - tj + gg) * 1024 + (16 * tj + c16) * 16;
    {
        const float xgn = norm(xg);
        float acc[8];
#pragma unroll
        for (int e = 0; e < 8; ++e) acc[e] = 0.f;
#pragma unroll
        for (int j = 0; j < 8; ++j) {
            const float xn = bitsf(__builtin_amdgcn_readlane(fbits(xgn), j));
#pragma unroll
            for (int d = 0; d < 4; ++d) { acc[2 * d] = fmaf(xn, bflo(gu[j][d]), acc[2 * d]); acc[2 * d + 1] = fmaf(xn, bfhi(gu[j][d]), acc[2 * d + 1]); }
            PIN8(acc);
            SB();
            if (j == QP_AT && w == 0) qp = ld_sc1(rWS, qpo, 0);
            kb[j] = kload(j);
            SB();
        }
        if (lane < 56) {
            LAS f32x4* gr = (LAS f32x4*)(L + L_GU + w * 1792 + lane * 32);
            gr[0] = f32x4{acc[0], acc[1], acc[2], acc[3]}; gr[1] = f32x4{acc[4], acc[5], acc[6], acc[7]};
        }
    }
    WG_BARRIER();
    if (tid < 112) {
        f32x4 s = {0.f, 0.f, 0.f, 0.f};
#pragma unroll
        for (int ww = 0; ww < 8; ++ww) s += *(LAS f32x4*)(L + L_GU + ww * 1792 + tid * 16);
        st_sc1(v4u{nosent(s[0]), nosent(s[1]), nosent(s[2]), nosent(s[3])}, rWS, tid * 16, obase + OB_F1);
    }
    if (tid == 448) __hip_atomic_store((gu64*)((unsigned char*)p.ws + WS_FLAG) + cid, E + 1, __ATOMIC_RELAXED, __HIP_MEMORY_SCOPE_AGENT);
    STAMP(3);
    if (w == 0) {
        if (__any(badtag(qp))) {
            unsigned spins = 0;
            do { REPOLL(); qp = ld_sc1(rWS, qpo, 0); RETRY(21); } while (__any(badtag(qp)) && ++spins < SPIN_CAP);
        }
        const float q = xrow_sum(bitsf((qp.y & 0xffff0000u) | (qp.w >> 16)));
        if (lane < 16) {
            const unsigned long long gr = ((unsigned long long)f2bf(q) << 48) | tag48;
            __hip_atomic_store((gu64*)((unsigned char*)p.ws + WS_QG) + 16 * cid + lane, gr, __ATOMIC_RELAXED, __HIP_MEMORY_SCOPE_AGENT);
        }
    }
    STAMP(14);
    SB();
#pragma unroll
    for (int j = 8; j < QG_AT; ++j) kb[j] = kload(j);
    SB();
    v4u qg = ld_sc1(rWS, lane * 16, WS_QG + g * 8192 + w * 1024);
    SB();
#pragma unroll
    for (int j = QG_AT; j < 24; ++j) kb[j] = kload(j);
    SB();
    {
        if (__any(badtag(qg))) {
            unsigned spins = 0;
            do { REPOLL(); qg = ld_sc1(rWS, lane * 16, WS_QG + g * 8192 + w * 1024); RETRY(13); } while (__any(badtag(qg)) && ++spins < SPIN_CAP);
        }
        *(LAS unsigned*)(L + L_Q + w * 256 + lane * 4) = (qg.y >> 16) | (qg.w & 0xffff0000u);
    }
    WG_BARRIER();
    v4u qf[2];
#pragma unroll
    for (int s = 0; s < 2; ++s) qf[s] = *(LAS v4u*)(L + L_Q + (c16 & 7) * 256 + (64 * s + 32 * (c16 >> 3) + 8 * gg) * 2);
    STAMP(4);
    SB();

    float sc[32];
    v4u vb[32];
    v4u flg;
#pragma unroll
    for (int t = 0; t < 4; ++t) {
        f32x4 dU[4];
#pragma unroll
        for (int U = 0; U < 4; ++U) {
            dU[U] = mfma32(kb[8 * t + 2 * U], qf[0], f32x4{0.f, 0.f, 0.f, 0.f});
            dU[U] = mfma32(kb[8 * t + 2 * U + 1], qf[1], dU[U]);
        }
        SB();
        if (t == FLG_AT) flg = ld_sc1(rWS, lane < 16 ? lane * 16 : OOB, WS_FLAG + w * 256);
        if (t < 1) {
#pragma unroll
            for (int j = 0; j < 8; ++j) kb[24 + j] = kload(24 + j);
        } else if (t < 3) {
#pragma unroll
            for (int j = 0; j < 8; ++j) vb[8 * (t - 1) + j] = vload(8 * (t - 1) + j);
        }
        SB();
#pragma unroll
        for (int h = 0; h < 2; ++h)
#pragma unroll
            for (int i = 0; i < 4; ++i) {
                auto sw = __builtin_amdgcn_permlane32_swap(fbits(dU[h][i]), fbits(dU[h + 2][i]), false, false);
                const float s = bitsf(sw[0]) + dpp<ROR8>(bitsf(sw[1]));
                sc[8 * t + 4 * h + i] = rbf(rbf(s) * 0.08838834764831845f);
            }
    }
    STAMP(5);
    float mx = sc[0];
#pragma unroll
    for (int i = 1; i < 32; ++i) mx = fmaxf(mx, sc[i]);
    mx = xrow_max(mx);
    float lsum = 0.f;
    v4u pf[4];
    {
        const float mneg = -mx * LOG2E;
#pragma unroll
        for (int t = 0; t < 4; ++t)
#pragma unroll
            for (int d = 0; d < 4; ++d) {
                const float p0 = __builtin_amdgcn_exp2f(fmaf(sc[8 * t + 2 * d], LOG2E, mneg)), p1 = __builtin_amdgcn_exp2f(fmaf(sc[8 * t + 2 * d + 1], LOG2E, mneg));
                lsum += p0 + p1;
                pf[t][d] = pk2(p0, p1);
            }
        lsum = xrow_sum(lsum);
    }
    {
        const unsigned el = (unsigned)(E + 1), eh = (unsigned)((E + 1) >> 32);
        auto bad = [&](v4u f) { return (lane < 16) & ((((f.x ^ el) | (f.z ^ el)) | ((f.y ^ eh) | (f.w ^ eh))) != 0u); };
        if (__any(bad(flg))) {
            unsigned spins = 0;
            do { REPOLL(); __builtin_amdgcn_s_sleep(2); flg = ld_sc1(rWS, lane < 16 ? lane * 16 : OOB, WS_FLAG + w * 256); } while (__any(bad(flg)) && ++spins < SPIN_CAP);
            RETRY(16);
        }
    }
    WG_BARRIER();
    SB();
    const int f1vo = (lane < 32 && (lane & 7) < 7) ? (16 * (4 * (w & 3) + (lane >> 3)) + 8 * (w >> 2) + (cid >> 5)) * OB_STRIDE + 56 * (cid & 31) + 8 * (lane & 7) : OOB;
    v2u f1 = ld_sc1_64(rWS, f1vo, WS_OUT + OB_F1);
    v4u wd[4];
    {
#pragma unroll
        for (int j = 0; j < 4; ++j) { const int row = 4 * gg + j; wd[j] = ld_nt(rWd, (14 * cid + (row < 14 ? row : 13)) * 2048 + c16 * 16, w * 256); }
    }
    SB();
    STAMP(6);

    f32x4 oa[8];
#pragma unroll
    for (int e = 0; e < 8; ++e) oa[e] = f32x4{0.f, 0.f, 0.f, 0.f};
    v4u wo[4];
    auto pv = [&](int t) {
#pragma unroll
        for (int d = 0; d < 4; ++d) {
            v4u lo, hi;
#pragma unroll
            for (int i = 0; i < 4; ++i) {
                lo[i] = __builtin_amdgcn_perm(vb[8 * t + 2 * i + 1][d], vb[8 * t + 2 * i][d], 0x05040100u);
                hi[i] = __builtin_amdgcn_perm(vb[8 * t + 2 * i + 1][d], vb[8 * t + 2 * i][d], 0x07060302u);
            }
            oa[2 * d] = mfma32(pf[t], lo, oa[2 * d]);
            oa[2 * d + 1] = mfma32(pf[t], hi, oa[2 * d + 1]);
        }
    };
    pv(0);
    SB();
#pragma unroll
    for (int j = 16; j < 24; ++j) vb[j] = vload(j);
    SB();
    pv(1);
    SB();
#pragma unroll
    for (int j = 24; j < 32; ++j) vb[j] = vload(j);
    SB();
    {
        auto bad = [&](v2u f) { return umax2(f.x, f.y) == SENT; };
        if (__any(bad(f1))) {
            unsigned spins = 0;
            do { REPOLL(); __builtin_amdgcn_s_sleep(2); f1 = ld_sc1_64(rWS, f1vo, WS_OUT + OB_F1); } while (__any(bad(f1)) && ++spins < SPIN_CAP);
            RETRY(17);
        }
        float a0 = bitsf(f1.x), a1 = bitsf(f1.y);
        a0 += dpp<ROR8>(a0); a1 += dpp<ROR8>(a1);
        auto s0 = __builtin_amdgcn_permlane16_swap(fbits(a0), fbits(a0), false, false); a0 = bitsf(s0[0]) + bitsf(s0[1]);
        auto s1 = __builtin_amdgcn_permlane16_swap(fbits(a1), fbits(a1), false, false); a1 = bitsf(s1[0]) + bitsf(s1[1]);
        if (lane < 8) { LAS float* f = (LAS float*)(L + L_F1 + w * 64 + lane * 8); f[0] = a0; f[1] = a1; }
    }
    WG_BARRIER();
    {
        unsigned hb[4];
#pragma unroll
        for (int j = 0; j < 4; ++j) {
            const LAS float* f = (const LAS float*)(L + L_F1) + 4 * gg + j;
            const float gv = rbf((f[0] + f[16]) + (f[32] + f[48])), uv = rbf((f[64] + f[80]) + (f[96] + f[112]));
            const float inner = 0.7978845608028654f * (gv + 0.044715f * gv * gv * gv);
            const float th = 1.0f - 2.0f / (1.0f + __expf(2.0f * inner));
            const float ge = rbf(0.5f * gv * (1.0f + th));
            hb[j] = (4 * gg + j) < 14 ? f2bf(ge * uv) : 0u;
        }
        const v4u y = gemv16(wd, v2u{hb[0] | (hb[1] << 16), hb[2] | (hb[3] << 16)}, lane);
        st_sc1(y, rWS, lane < 32 ? (128 * w + 8 * c16 + 4 * gg) * 4 : OOB, obase + OB_F3);
    }
    STAMP(7);
    SB();
    pv(2);
    SB();
#pragma unroll
    for (int j = 0; j < 4; ++j) wo[j] = ld_nt(rWo, (16 * cid + 4 * gg + j) * 2048 + c16 * 16, w * 256);
    SB();
    pv(3);
    STAMP(8);

    if (lane < 32) {
#pragma unroll
        for (int i = 0; i < 4; ++i) {
            LAS f32x4* m = (LAS f32x4*)(L + L_MRG + w * 4096 + (4 * gg + i) * 512 + c16 * 32);
            m[0] = f32x4{oa[0][i], oa[1][i], oa[2][i], oa[3][i]}; m[1] = f32x4{oa[4][i], oa[5][i], oa[6][i], oa[7][i]};
        }
    }
    if (lane < 8) { LAS float* ml = (LAS float*)(L + L_ML + w * 64 + lane * 8); ml[0] = mx; ml[1] = lsum; }
    WG_BARRIER();
    {
        float mw[8], M = -INFINITY;
#pragma unroll
        for (int ww = 0; ww < 8; ++ww) { mw[ww] = *(LAS float*)(L + L_ML + ww * 64 + w * 8); M = fmaxf(M, mw[ww]); }
        float Ls = 0.f; f32x4 o = {0.f, 0.f, 0.f, 0.f};
#pragma unroll
        for (int ww = 0; ww < 8; ++ww) {
            const float f = __builtin_amdgcn_exp2f((mw[ww] - M) * LOG2E);
            Ls = fmaf(f, *(LAS float*)(L + L_ML + ww * 64 + w * 8 + 4), Ls);
            o += f * *(LAS f32x4*)(L + L_MRG + ww * 4096 + w * 512 + (lane & 31) * 16);
        }
        const v4u rec = lane < 32 ? v4u{nosent(o[0]), nosent(o[1]), nosent(o[2]), nosent(o[3])} : v4u{nosent(M), nosent(Ls), 0u, 0u};
        const int vo = lane < 32 ? (8 * w + (lane >> 2)) * 80 + (lane & 3) * 16 : lane < 40 ? (8 * w + (lane - 32)) * 80 + 64 : OOB;
        st_sc1(rec, rWS, vo, obase + OB_A1);
    }
    STAMP(9);
    const int ocol = 128 * xl + 4 * ii;
    if (w == 1) {
        v4u f[4];
        auto ldf = [&]() {
#pragma unroll
            for (int t = 0; t < 4; ++t) f[t] = ld_sc1(rWS, (64 * t + lane) * OB_STRIDE, WS_OUT + OB_F3 + ocol * 4);
            SB(); };
        auto bad = [&]() { return umax2(umax2(umax4(f[0]), umax4(f[1])), umax2(umax4(f[2]), umax4(f[3]))) == SENT; };
        ldf();
        if (__any(bad())) { unsigned spins = 0; do { REPOLL(); __builtin_amdgcn_s_sleep(1); ldf(); RETRY(20); } while (__any(bad()) && ++spins < SPIN_CAP); }
        float y[4];
#pragma unroll
        for (int e = 0; e < 4; ++e) y[e] = wave_sum((bitsf(f[0][e]) + bitsf(f[1][e])) + (bitsf(f[2][e]) + bitsf(f[3][e])));
        if (lane == 0) {
            f32x4 gt;
#pragma unroll
            for (int e = 0; e < 4; ++e) { const float v = rbf(y[e]); gt[e] = rbf(v / (1.0f + __expf(-v))); }
            *(LAS f32x4*)(L + L_O + 64) = gt;
        }
    }
    if (w == 0) {
        v4u a[4], ml;
        const int gb = WS_OUT + 64 * g * OB_STRIDE + OB_A1 + r * 80;
        auto lda = [&]() {
#pragma unroll
            for (int t = 0; t < 4; ++t) a[t] = ld_sc1(rWS, (16 * t + (lane >> 2)) * OB_STRIDE + (lane & 3) * 16, gb);
            ml = ld_sc1(rWS, lane * OB_STRIDE + 64, gb);
            SB(); };
        auto bad = [&]() { return umax2(umax2(umax2(umax4(a[0]), umax4(a[1])), umax2(umax4(a[2]), umax4(a[3]))), umax4(ml)) == SENT; };
        lda();
        if (__any(bad())) { unsigned spins = 0; do { REPOLL(); lda(); RETRY(18); } while (__any(bad()) && ++spins < SPIN_CAP); }
        const float m = bitsf(ml.x), M = wave_max(m), wt = __builtin_amdgcn_exp2f((m - M) * LOG2E);
        const float Ls = wave_sum(wt * bitsf(ml.y));
        float o[4] = {0.f, 0.f, 0.f, 0.f};
#pragma unroll
        for (int t = 0; t < 4; ++t) {
            const float wv = bitsf((unsigned)__builtin_amdgcn_ds_bpermute((16 * t + (lane >> 2)) * 4, (int)fbits(wt)));
#pragma unroll
            for (int e = 0; e < 4; ++e) o[e] = fmaf(wv, bitsf(a[t][e]), o[e]);
        }
#pragma unroll
        for (int e = 0; e < 4; ++e) { float v = o[e]; v += dpp<ROR4>(v); v += dpp<ROR8>(v); o[e] = xrow_sum(v) / Ls; }
        if (lane < 4) *(LAS v2u*)(L + L_O + lane * 8) = v2u{pk2(o[0], o[1]), pk2(o[2], o[3])};
    }
    WG_BARRIER();
    STAMP(10);
    {
        const v2u oa2 = *(LAS v2u*)(L + L_O + gg * 8);
        const v4u y = gemv16(wo, oa2, lane);
        st_sc1(y, rWS, lane < 32 ? (128 * w + 8 * c16 + 4 * gg) * 4 : OOB, obase + OB_A3);
    }
    STAMP(11);
    if (w == 0) {
        v4u f[4];
        auto ldf = [&]() {
#pragma unroll
            for (int t = 0; t < 4; ++t) f[t] = ld_sc1(rWS, (64 * t + lane) * OB_STRIDE, WS_OUT + OB_A3 + ocol * 4);
            SB(); };
        auto bad = [&]() { return umax2(umax2(umax4(f[0]), umax4(f[1])), umax2(umax4(f[2]), umax4(f[3]))) == SENT; };
        const v2u xo = __builtin_amdgcn_raw_buffer_load_b64(rX, ocol * 2, 0, 0);
        ldf();
        if (__any(bad())) { unsigned spins = 0; do { REPOLL(); ldf(); RETRY(19); } while (__any(bad()) && ++spins < SPIN_CAP); }
        float y[4];
#pragma unroll
        for (int e = 0; e < 4; ++e) y[e] = wave_sum((bitsf(f[0][e]) + bitsf(f[1][e])) + (bitsf(f[2][e]) + bitsf(f[3][e])));
        if (lane == 0) {
            const f32x4 gt = *(LAS f32x4*)(L + L_O + 64);
            const float xv[4] = {bflo(xo.x), bfhi(xo.x), bflo(xo.y), bfhi(xo.y)};
            float o[4];
#pragma unroll
            for (int e = 0; e < 4; ++e) o[e] = xv[e] + rbf(rbf(y[e]) * gt[e]);
            *(GAS v2u*)((unsigned char*)p.out + ocol * 2) = v2u{pk2(o[0], o[1]), pk2(o[2], o[3])};
            if (b == 0) __hip_atomic_store((gu64*)p.ws, E + 1, __ATOMIC_RELAXED, __HIP_MEMORY_SCOPE_AGENT);
        }
    }
    STAMP(12);
}
}

extern "C" void kernel_launch(void* const* d_in, const int* in_sizes, int n_in, void* d_out, int out_size, void* d_ws, size_t ws_size, hipStream_t stream) {
    (void)in_sizes; (void)n_in; (void)out_size; (void)ws_size;
    Params p;
    p.x = d_in[0]; p.kc = d_in[1]; p.vc = d_in[2]; p.wq = d_in[3]; p.wo = d_in[4];
    p.wg = d_in[6]; p.wu = d_in[7]; p.wd = d_in[8];
    p.out = d_out; p.ws = d_ws;
    (void)hipFuncSetAttribute((const void*)fused_decode, hipFuncAttributeMaxDynamicSharedMemorySize, LDS_BYTES);
    hipLaunchKernelGGL(fused_decode, dim3(NCU), dim3(NTHR), LDS_BYTES, stream, p);
}
```

```cpp
#include <hip/hip_runtime.h>
#include <hip/hip_bf16.h>
#include <stdint.h>

#define DIAG 0
#define QP_AT 6
#define QG_AT 20
#define QC_AT 26
#define FC_AT 30
#define FL_AT 18
#define F1_AT 24
#define VE 0
#define QG_TIME 850
#define FL_TIME 760
#define EARLYFILL 0
#define EARLYAT 0
#define FILLDLY 4
#define NLATE 7
#define LATE 0
#define STAG 6
#define NPOLL 3
#define F3W 0
#define GU_EARLY 3

namespace {
#define GAS __attribute__((address_space(1)))
#define LAS __attribute__((address_space(3)))
typedef unsigned v4u __attribute__((ext_vector_type(4)));
typedef unsigned v2u __attribute__((ext_vector_type(2)));
typedef float f32x4 __attribute__((ext_vector_type(4)));
typedef short bf16x8 __attribute__((ext_vector_type(8)));
typedef short bf16x4 __attribute__((ext_vector_type(4)));
typedef __amdgpu_buffer_rsrc_t rsrc_t;
typedef GAS unsigned long long gu64;

constexpr int DM = 1024, SK = 65536, DF = 3584;
constexpr int NCU = 256, NW = 8, NTHR = NW * 64;
constexpr int AUX_NT = 2, AUX_SC1 = 16;
constexpr int RSRC_W3 = 0x00020000;
constexpr unsigned SENT = 0xFFFFFFFFu;
constexpr unsigned SPIN_CAP = 1u << 14, LDS_SPIN_CAP = 1u << 20;

constexpr unsigned WS_EPOCH = 0;
constexpr unsigned WS_FLAG1 = 256;
constexpr unsigned WS_QG = WS_FLAG1 + 2048;
constexpr unsigned WS_QP = WS_QG + 32768;
constexpr unsigned WS_ML = WS_QP + 256 * 1024;
constexpr unsigned WS_CNT = WS_ML + 32 * 512;
constexpr unsigned WS_OUT = WS_CNT + 16 * 128;
constexpr unsigned OB_A1 = 0, OB_AF = 4096, OB_F1 = OB_AF + 4096, OB_CLR = OB_F1 + 1792, OB_BYTES = OB_CLR, OB_STRIDE = 10112;
constexpr int NSTAT = 3, NDYN = 256;
constexpr unsigned WS_END = WS_OUT + NCU * OB_STRIDE;
constexpr unsigned WS_DIAG = 8u << 20;
static_assert(WS_OUT % 128 == 0 && OB_BYTES <= OB_STRIDE && OB_STRIDE % 128 == 0 && WS_END < WS_DIAG, "ws map");

constexpr unsigned L_XN = 0;
constexpr unsigned L_RED = 4096;
constexpr unsigned L_Q = L_RED + 2048;
constexpr unsigned L_O = L_Q + 2048;
constexpr unsigned L_ML = L_O + 256;
constexpr unsigned L_GU = L_ML + 512;
constexpr unsigned L_MRG = L_GU + 8 * 448 * 4;
constexpr unsigned L_END = L_MRG + 32768;
constexpr unsigned LDS_BYTES = 96 * 1024;
static_assert(L_END <= LDS_BYTES && L_GU % 16 == 0 && L_MRG % 16 == 0, "lds map");

__device__ __forceinline__ float bf2f(unsigned h) { return __builtin_bit_cast(float, h << 16); }
__device__ __forceinline__ float bflo(unsigned u) { return __builtin_bit_cast(float, u << 16); }
__device__ __forceinline__ float bfhi(unsigned u) { return __builtin_bit_cast(float, u & 0xffff0000u); }
__device__ __forceinline__ unsigned f2bf(float f) { return (unsigned)__builtin_bit_cast(unsigned short, (__bf16)f); }
__device__ __forceinline__ float rbf(float f) { return bf2f(f2bf(f)); }
__device__ __forceinline__ unsigned pk2(float lo, float hi) {
    typedef __bf16 b2 __attribute__((ext_vector_type(2))); typedef float f2 __attribute__((ext_vector_type(2)));
    return __builtin_bit_cast(unsigned, __builtin_convertvector((f2){lo, hi}, b2)); }
__device__ __forceinline__ unsigned fbits(float f) { return __builtin_bit_cast(unsigned, f); }
__device__ __forceinline__ float bitsf(unsigned u) { return __builtin_bit_cast(float, u); }
__device__ __forceinline__ unsigned nosent(float f) { const unsigned u = fbits(f); return u < 0xFFFFFFFEu ? u : 0xFFFFFFFEu; }
template <int CTRL> __device__ __forceinline__ float dpp(float x) { return bitsf((unsigned)__builtin_amdgcn_mov_dpp((int)fbits(x), CTRL, 0xf, 0xf, true)); }
constexpr int ROR1 = 0x121, ROR2 = 0x122, ROR4 = 0x124, ROR8 = 0x128;
__device__ __forceinline__ float xrow_sum(float x) {
    auto s = __builtin_amdgcn_permlane16_swap(fbits(x), fbits(x), false, false); x = bitsf(s[0]) + bitsf(s[1]);
    auto t = __builtin_amdgcn_permlane32_swap(fbits(x), fbits(x), false, false); return bitsf(t[0]) + bitsf(t[1]); }
__device__ __forceinline__ float xrow_max(float x) {
    auto s = __builtin_amdgcn_permlane16_swap(fbits(x), fbits(x), false, false); x = fmaxf(bitsf(s[0]), bitsf(s[1]));
    auto t = __builtin_amdgcn_permlane32_swap(fbits(x), fbits(x), false, false); return fmaxf(bitsf(t[0]), bitsf(t[1])); }
__device__ __forceinline__ float wave_sum(float x) { x += dpp<ROR1>(x); x += dpp<ROR2>(x); x += dpp<ROR4>(x); x += dpp<ROR8>(x); return xrow_sum(x); }
__device__ __forceinline__ float wave_max(float x) { x = fmaxf(x, dpp<ROR1>(x)); x = fmaxf(x, dpp<ROR2>(x)); x = fmaxf(x, dpp<ROR4>(x)); x = fmaxf(x, dpp<ROR8>(x)); return xrow_max(x); }
__device__ __forceinline__ unsigned claim_issue(const void* word) {
    unsigned ret; unsigned long long ex;
    asm volatile("s_mov_b64 %1, exec\n\ts_mov_b64 exec, 1\n\tglobal_atomic_add %0, %2, %3, %4 sc0\n\ts_mov_b64 exec, %1"
                 : "=&v"(ret), "=&s"(ex) : "v"(0u), "v"(1u), "s"(word) : "memory");
    return ret; }
template <int N> __device__ __forceinline__ unsigned claim_wait(unsigned ret) {
    asm volatile("s_waitcnt vmcnt(%1)\n\ts_nop 0" : "+v"(ret) : "i"(N) : "memory");
    return __builtin_amdgcn_readfirstlane(ret); }
template <int I> __device__ __forceinline__ float headbc(float x) {
    int a = __builtin_amdgcn_update_dpp(0, (int)fbits(x), 0x150 + I, 0x5, 0xf, false);
    a = __builtin_amdgcn_update_dpp(a, (int)fbits(x), 0x154 + I, 0xA, 0xf, false);
    return bitsf((unsigned)a); }
__device__ __forceinline__ float bperm(int srclane, float v) { return bitsf((unsigned)__builtin_amdgcn_ds_bpermute(srclane * 4, (int)fbits(v))); }
__device__ __forceinline__ rsrc_t mk_rsrc(const void* p, unsigned bytes) { return __builtin_amdgcn_make_buffer_rsrc((void*)p, 0, (int)bytes, RSRC_W3); }
__device__ __forceinline__ v4u ld_nt(rsrc_t r, int voff, int soff) { return __builtin_amdgcn_raw_buffer_load_b128(r, voff, soff, AUX_NT); }
__device__ __forceinline__ v4u ld_sc1(rsrc_t r, int voff, int soff) { return __builtin_amdgcn_raw_buffer_load_b128(r, voff, soff, AUX_SC1); }
__device__ __forceinline__ v2u ld_sc1_64(rsrc_t r, int voff, int soff) { return __builtin_amdgcn_raw_buffer_load_b64(r, voff, soff, AUX_SC1); }
__device__ __forceinline__ void st_sc1(v4u v, rsrc_t r, int voff) { __builtin_amdgcn_raw_buffer_store_b128(v, r, voff, 0, AUX_SC1); }
__device__ __forceinline__ void st_sc1_64(v2u v, rsrc_t r, int voff) { __builtin_amdgcn_raw_buffer_store_b64(v, r, voff, 0, AUX_SC1); }
__device__ __forceinline__ unsigned umax4(v4u v) { const unsigned a = v.x > v.y ? v.x : v.y, b = v.z > v.w ? v.z : v.w; return a > b ? a : b; }
__device__ __forceinline__ unsigned umax2(unsigned a, unsigned b) { return a > b ? a : b; }
#define PIN8(a) asm volatile("" : "+v"(a[0]), "+v"(a[1]), "+v"(a[2]), "+v"(a[3]), "+v"(a[4]), "+v"(a[5]), "+v"(a[6]), "+v"(a[7]))
__device__ __forceinline__ f32x4 mfma32(v4u a, v4u b, f32x4 c) { return __builtin_amdgcn_mfma_f32_16x16x32_bf16(__builtin_bit_cast(bf16x8, a), __builtin_bit_cast(bf16x8, b), c, 0, 0, 0); }
__device__ __forceinline__ f32x4 mfma16(v2u a, v2u b, f32x4 c) { return __builtin_amdgcn_mfma_f32_16x16x16bf16_1k(__builtin_bit_cast(bf16x4, a), __builtin_bit_cast(bf16x4, b), c, 0, 0, 0); }
#define SB() __builtin_amdgcn_sched_barrier(0)
#define PRIO(n) __builtin_amdgcn_s_setprio(n)
#define REPOLL() asm volatile("" ::: "memory")
#define WG_BARRIER() asm volatile("s_waitcnt lgkmcnt(0)\n\ts_barrier" ::: "memory")
#define DRAIN() asm volatile("s_waitcnt vmcnt(0)" ::: "memory")
constexpr int OOB = (int)0x80000000u;
constexpr float LOG2E = 1.4426950408889634f;

#define RETRY(k) do { } while (0)
#define STAMP(k) do { } while (0)

struct Params {
    const void* x; const void* kc; const void* vc; const void* wq; const void* wo; const void* wg; const void* wu; const void* wd;
    void* out; void* ws;
};

__device__ __forceinline__ void mfma_rows4(const v4u (&wv)[4], v2u a, f32x4 (&acc)[8]) {
#pragma unroll
    for (int d = 0; d < 4; ++d) {
        v2u lo, hi;
        lo.x = __builtin_amdgcn_perm(wv[1][d], wv[0][d], 0x05040100u); lo.y = __builtin_amdgcn_perm(wv[3][d], wv[2][d], 0x05040100u);
        hi.x = __builtin_amdgcn_perm(wv[1][d], wv[0][d], 0x07060302u); hi.y = __builtin_amdgcn_perm(wv[3][d], wv[2][d], 0x07060302u);
        acc[2 * d] = mfma16(a, lo, f32x4{0.f, 0.f, 0.f, 0.f});
        acc[2 * d + 1] = mfma16(a, hi, f32x4{0.f, 0.f, 0.f, 0.f});
    }
}

__global__ void __launch_bounds__(NTHR, 2) fused_decode(Params p) {
    extern __shared__ __attribute__((aligned(16))) unsigned char smem[];
    LAS unsigned char* L = (LAS unsigned char*)smem;
    const int tid = threadIdx.x, lane = tid & 63, w = __builtin_amdgcn_readfirstlane(tid >> 6);
    const int b = blockIdx.x, xl = b & 7, ii = b >> 3, g = ii >> 3, r = 8 * xl + (ii & 7), cid = 64 * g + r;
    const int gg = lane >> 4, c16 = lane & 15;
    STAMP(0);
    const unsigned long long T0 = __builtin_amdgcn_s_memrealtime();
    const rsrc_t rX = mk_rsrc(p.x, DM * 2), rK = mk_rsrc(p.kc, 4u * SK * 256), rV = mk_rsrc(p.vc, 4u * SK * 256);
    const rsrc_t rWq = mk_rsrc(p.wq, DM * 4096 * 2), rWo = mk_rsrc(p.wo, 4096 * DM * 2), rWd = mk_rsrc(p.wd, DF * DM * 2);
    const rsrc_t rWS = mk_rsrc(p.ws, WS_END);
    const int ks = cid >> 4, cg = cid & 15;
    const rsrc_t rGU = mk_rsrc((cg >> 3) ? p.wu : p.wg, DM * DF * 2);
    const int obase = WS_OUT + cid * OB_STRIDE;
    const int pool = 4 * g + ((ii & 7) >> 1);
    const int tj = ii & 3;

    v4u wq[4], gu[8];
    auto wq_requests = [&]() {
        const int vo = (256 * tj + 8 * w + (lane >> 3)) * 8192 + (16 * (cid - tj)) * 2 + (lane & 7) * 16;
#pragma unroll
        for (int t = 0; t < 4; ++t) wq[t] = ld_nt(rWq, vo, t * 64 * 8192);
        SB();
        asm volatile("s_barrier" ::: "memory");
        SB();
    };
    auto gu_requests_a = [&]() {
        const int go = lane < 56 ? (448 * (cg & 7) + 8 * lane) * 2 : OOB;
        const int so = (64 * ks + 8 * w) * (DF * 2);
#pragma unroll
        for (int j = 0; j < GU_EARLY; ++j) gu[j] = ld_nt(rGU, go, so + j * (DF * 2));
        SB();
    };
    auto gu_requests_b = [&]() {
        const int go = lane < 56 ? (448 * (cg & 7) + 8 * lane) * 2 : OOB;
        const int so = (64 * ks + 8 * w) * (DF * 2);
        st_sc1(v4u{SENT, SENT, SENT, SENT}, rWS, obase + tid * 16);
        st_sc1(v4u{SENT, SENT, SENT, SENT}, rWS, tid < (int)(OB_CLR / 16) - NTHR ? obase + (NTHR + tid) * 16 : OOB);
        st_sc1_64(tid == NTHR - 9 ? v2u{0u, 0u} : v2u{SENT, SENT}, rWS,
                  tid >= NTHR - 8 ? WS_ML + (8 * g + tid - (NTHR - 8)) * 512 + r * 8 : (tid == NTHR - 9 && xl == 0 && (ii & 1) == 0) ? WS_CNT + pool * 128 : OOB);
        SB();
#pragma unroll
        for (int j = GU_EARLY; j < 8; ++j) gu[j] = ld_nt(rGU, go, so + j * (DF * 2));
        SB();
    };
    auto gu_requests = [&]() {
        const int go = lane < 56 ? (448 * (cg & 7) + 8 * lane) * 2 : OOB;
        const int so = (64 * ks + 8 * w) * (DF * 2);
#pragma unroll
        for (int j = 0; j < GU_EARLY; ++j) gu[j] = ld_nt(rGU, go, so + j * (DF * 2));
        SB();
        st_sc1(v4u{SENT, SENT, SENT, SENT}, rWS, obase + tid * 16);
        st_sc1(v4u{SENT, SENT, SENT, SENT}, rWS, tid < (int)(OB_CLR / 16) - NTHR ? obase + (NTHR + tid) * 16 : OOB);
        st_sc1_64(tid == NTHR - 9 ? v2u{0u, 0u} : v2u{SENT, SENT}, rWS,
                  tid >= NTHR - 8 ? WS_ML + (8 * g + tid - (NTHR - 8)) * 512 + r * 8 : (tid == NTHR - 9 && xl == 0 && (ii & 1) == 0) ? WS_CNT + pool * 128 : OOB);
        SB();
#pragma unroll
        for (int j = GU_EARLY; j < 8; ++j) gu[j] = ld_nt(rGU, go, so + j * (DF * 2));
        SB();
    };
    if (w == 0) {
        const v4u xa = __builtin_amdgcn_raw_buffer_load_b128(rX, lane * 32, 0, 0), xb = __builtin_amdgcn_raw_buffer_load_b128(rX, lane * 32 + 16, 0, 0);
        const unsigned long long E0 = __hip_atomic_load((gu64*)p.ws, __ATOMIC_RELAXED, __HIP_MEMORY_SCOPE_AGENT);
        if (lane == 0) { *(LAS unsigned*)(L + L_O + 96) = 0u; *(LAS unsigned*)(L + L_O + 100) = 0u; *(LAS unsigned*)(L + L_O + 136) = 0u; *(LAS unsigned*)(L + L_O + 140) = 0u; *(LAS unsigned*)(L + L_O + 144) = 0u; *(LAS unsigned*)(L + L_O + 148) = 0u; }
        asm volatile("s_barrier" ::: "memory");
        SB();
        wq_requests();
        *(LAS v4u*)(L + L_XN + lane * 32) = xa; *(LAS v4u*)(L + L_XN + lane * 32 + 16) = xb;
        float xv[16];
#pragma unroll
        for (int d = 0; d < 4; ++d) { xv[2 * d] = bflo(xa[d]); xv[2 * d + 1] = bfhi(xa[d]); xv[8 + 2 * d] = bflo(xb[d]); xv[8 + 2 * d + 1] = bfhi(xb[d]); }
        float s = 0.f;
#pragma unroll
        for (int d = 0; d < 16; ++d) s += xv[d];
        const float mean = wave_sum(s) * (1.0f / DM);
        float v = 0.f;
#pragma unroll
        for (int d = 0; d < 16; ++d) { const float t = xv[d] - mean; v = fmaf(t, t, v); }
        const float var = rbf(wave_sum(v) * (1.0f / DM));
        if (lane == 0) {
            *(LAS float*)(L + L_XN + 2048) = rbf(mean); *(LAS float*)(L + L_XN + 2052) = rbf(sqrtf(rbf(var + 1e-5f)));
            *(LAS unsigned long long*)(L + L_O + 80) = E0 + 1;
        }
        WG_BARRIER();
        gu_requests();
    } else {
        asm volatile("s_barrier" ::: "memory");
        SB();
        wq_requests();
        gu_requests_a();
        WG_BARRIER();
        gu_requests_b();
    }
    STAMP(1);
    const unsigned long long E1 = *(LAS unsigned long long*)(L + L_O + 80);
    const unsigned el = (unsigned)E1, eh = (unsigned)(E1 >> 32);
    const unsigned tl = el, th = eh & 0xffffu;
    auto badtag = [&](v4u q) { return (((q.x ^ tl) | (q.z ^ tl)) | (((q.y ^ th) | (q.w ^ th)) & 0xffffu)) != 0u; };
    const float mean_b = *(LAS float*)(L + L_XN + 2048), sd_b = *(LAS float*)(L + L_XN + 2052);
    auto xnorm = [&](int i) -> float { return rbf(rbf(bf2f(*(LAS unsigned short*)(L + L_XN + i * 2)) - mean_b) / sd_b); };
    SB();

    const int kkd = cid >> 1, nn = cid & 1, kk = r >> 1;
    v4u wd[4], wo[4];
    auto wd_requests = [&]() {
#pragma unroll
        for (int j = 0; j < 4; ++j) { const int row = 8 * j + 2 * gg + ((lane >> 3) & 1); wd[j] = ld_nt(rWd, row < 28 ? (28 * kkd + row) * 2048 + (lane & 7) * 16 : OOB, (512 * nn + 64 * w) * 2); } };
    auto wo_requests = [&]() {
#pragma unroll
        for (int j = 0; j < 4; ++j) wo[j] = ld_nt(rWo, (1024 * g + 32 * kk + 8 * j + 2 * gg + ((lane >> 3) & 1)) * 2048 + (lane & 7) * 16, (512 * nn + 64 * w) * 2); };
    if (EARLYAT == 0) { if (EARLYFILL & 1) wd_requests(); if (EARLYFILL & 2) wo_requests(); SB(); }

    {
        float acc[8];
#pragma unroll
        for (int e = 0; e < 8; ++e) acc[e] = 0.f;
#pragma unroll
        for (int t = 0; t < 4; ++t) { const float xn = xnorm(256 * tj + 64 * t + 8 * w + (lane >> 3));
#pragma unroll
            for (int d = 0; d < 4; ++d) { acc[2 * d] = fmaf(xn, bflo(wq[t][d]), acc[2 * d]); acc[2 * d + 1] = fmaf(xn, bfhi(wq[t][d]), acc[2 * d + 1]); } }
#pragma unroll
        for (int e = 0; e < 8; ++e) { float v = acc[e]; v += dpp<ROR8>(v); acc[e] = xrow_sum(v); }
        if (lane < 8) {
            LAS f32x4* red = (LAS f32x4*)(L + L_RED + w * 256 + lane * 32);
            red[0] = f32x4{acc[0], acc[1], acc[2], acc[3]}; red[1] = f32x4{acc[4], acc[5], acc[6], acc[7]};
        }
    }
    WG_BARRIER();
    if (w == 0) {
        float q = 0.f;
#pragma unroll
        for (int ww = 0; ww < 8; ++ww) q += ((LAS float*)(L + L_RED))[ww * 64 + lane];
        const unsigned u = fbits(q);
        st_sc1(v4u{tl, th | (u & 0xffff0000u), tl, th | (u << 16)}, rWS, WS_QP + cid * 1024 + lane * 16);
    }
    STAMP(2);
    SB();

    const int kvo = (4 * (lane & 3) + ((lane >> 2) & 1)) * 256 + ((lane >> 3) & 1) * 64 + gg * 16;
    const int tbase = g * (SK * 256) + (8 * r + w) * 8192;
    auto kload = [&](int j) -> v4u { const int t = j >> 3, U = (j >> 1) & 3, s = j & 1; return ld_nt(rK, kvo, tbase + t * (512 * 8192) + (16 * (U & 1) + 2 * (U >> 1)) * 256 + s * 128); };
    auto vload = [&](int j) -> v4u { const int t = j >> 3, jj = j & 7; return ld_nt(rV, lane * 16, tbase + t * (512 * 8192) + jj * 1024); };
    v4u kb[8 * NSTAT];
    v4u qp;
    const int qpo = WS_QP + (cid - tj + gg) * 1024 + (16 * tj + c16) * 16;
    {
        const float xgn = xnorm(64 * ks + 8 * w + (lane & 7));
        float acc[8];
#pragma unroll
        for (int e = 0; e < 8; ++e) acc[e] = 0.f;
#pragma unroll
        for (int j = 0; j < 8; ++j) {
            const float xn = bitsf(__builtin_amdgcn_readlane(fbits(xgn), j));
#pragma unroll
            for (int d = 0; d < 4; ++d) { acc[2 * d] = fmaf(xn, bflo(gu[j][d]), acc[2 * d]); acc[2 * d + 1] = fmaf(xn, bfhi(gu[j][d]), acc[2 * d + 1]); }
            PIN8(acc);
            SB();
            if (j == QP_AT && w == 0) qp = ld_sc1(rWS, qpo, 0);
            kb[j] = kload(j);
            SB();
        }
        if (lane < 56) {
            LAS f32x4* gr = (LAS f32x4*)(L + L_GU + w * 1792 + lane * 32);
            gr[0] = f32x4{acc[0], acc[1], acc[2], acc[3]}; gr[1] = f32x4{acc[4], acc[5], acc[6], acc[7]};
        }
    }
    if (EARLYAT == 1) { if (EARLYFILL & 1) wd_requests(); if (EARLYFILL & 2) wo_requests(); SB(); }
    WG_BARRIER();
    STAMP(22);
    PRIO(3);
    if (w == 0) {
        if (__any(badtag(qp))) {
            unsigned spins = 0;
            do { REPOLL(); qp = ld_sc1(rWS, qpo, 0); RETRY(21); } while (__any(badtag(qp)) && ++spins < SPIN_CAP);
        }
        const float q = xrow_sum(bitsf((qp.y & 0xffff0000u) | (qp.w >> 16)));
        if (lane < 16) st_sc1_64(v2u{tl, th | (f2bf(q) << 16)}, rWS, WS_QG + (16 * cid + lane) * 8);
    } else if (w == 1) {
#pragma unroll
        for (int h = 0; h < 2; ++h) {
            const int u = 64 * h + lane;
            f32x4 s = {0.f, 0.f, 0.f, 0.f};
#pragma unroll
            for (int ww = 0; ww < 8; ++ww) s += *(LAS f32x4*)(L + L_GU + ww * 1792 + (u < 112 ? u : 0) * 16);
            st_sc1(v4u{nosent(s[0]), nosent(s[1]), nosent(s[2]), nosent(s[3])}, rWS, u < 112 ? obase + OB_F1 + u * 16 : OOB);
        }
    } else if (w == 2) {
        if (lane == 0) st_sc1_64(v2u{el, eh}, rWS, WS_FLAG1 + cid * 8);
    }
    STAMP(14);
    SB();
    volatile LAS unsigned* vcount = (volatile LAS unsigned*)(L + L_O + 96);
    volatile LAS unsigned* hready = (volatile LAS unsigned*)(L + L_O + 100);
    volatile LAS unsigned* qready = (volatile LAS unsigned*)(L + L_O + 136);
    v4u vb[8 * NSTAT];
    auto krest = [&]() {
#pragma unroll
        for (int j = 8; j < 8 * NSTAT; ++j) kb[j] = kload(j);
        SB();
        PRIO(1);
#pragma unroll
        for (int j = 0; j < VE; ++j) vb[j] = vload(j);
        SB(); };
    if (w == 0) {
        PRIO(3);
        v4u qg[8];
        auto ldq = [&]() {
#pragma unroll
            for (int h = 0; h < 8; ++h) qg[h] = ld_sc1(rWS, lane * 16, WS_QG + g * 8192 + h * 1024);
            SB(); };
        auto badq = [&]() { bool bd = false;
#pragma unroll
            for (int h = 0; h < 8; ++h) bd |= badtag(qg[h]);
            return bd; };
        while (__builtin_amdgcn_s_memrealtime() - T0 < QG_TIME) __builtin_amdgcn_s_sleep(4);
        ldq();
        if (__any(badq())) { unsigned spins = 0; do { REPOLL(); __builtin_amdgcn_s_sleep(2); ldq(); RETRY(13); } while (__any(badq()) && ++spins < SPIN_CAP); }
#pragma unroll
        for (int h = 0; h < 8; ++h) *(LAS unsigned*)(L + L_Q + h * 256 + lane * 4) = (qg[h].y >> 16) | (qg[h].w & 0xffff0000u);
        asm volatile("s_waitcnt lgkmcnt(0)" ::: "memory");
        if (lane == 0) *qready = 1u;
        STAMP(23);
        PRIO(2);
        krest();
    } else if (w == 1) {
        PRIO(3);
        v4u fl0, fl1;
        auto ldfl = [&]() { fl0 = ld_sc1(rWS, lane * 16, WS_FLAG1); fl1 = ld_sc1(rWS, lane * 16, WS_FLAG1 + 1024); SB(); };
        auto badflags = [&]() { return ((((fl0.x ^ el) | (fl0.z ^ el)) | ((fl0.y ^ eh) | (fl0.w ^ eh))) | (((fl1.x ^ el) | (fl1.z ^ el)) | ((fl1.y ^ eh) | (fl1.w ^ eh)))) != 0u; };
        while (__builtin_amdgcn_s_memrealtime() - T0 < FL_TIME) __builtin_amdgcn_s_sleep(4);
        ldfl();
        if (__any(badflags())) { unsigned spins = 0; do { REPOLL(); __builtin_amdgcn_s_sleep(2); ldfl(); RETRY(17); } while (__any(badflags()) && ++spins < SPIN_CAP); }
        REPOLL();
        v4u f1[4];
        auto ldf1 = [&]() {
#pragma unroll
            for (int t = 0; t < 4; ++t)
                f1[t] = ld_sc1(rWS, (lane & 7) < 7 ? (16 * (8 * (t & 1) + (lane >> 3)) + 8 * (t >> 1) + (kkd >> 4)) * OB_STRIDE + 112 * (kkd & 15) + 16 * (lane & 7) : OOB, WS_OUT + OB_F1);
            SB(); };
        auto badf1 = [&]() { return umax2(umax2(umax4(f1[0]), umax4(f1[1])), umax2(umax4(f1[2]), umax4(f1[3]))) == SENT; };
        ldf1();
        if (__any(badf1())) { unsigned spins = 0; do { REPOLL(); ldf1(); RETRY(17); } while (__any(badf1()) && ++spins < SPIN_CAP); }
#pragma unroll
        for (int e = 0; e < 4; ++e) {
            float a = bitsf(f1[0][e]) + bitsf(f1[1][e]); a += dpp<ROR8>(a); const float gv = rbf(xrow_sum(a));
            float c = bitsf(f1[2][e]) + bitsf(f1[3][e]); c += dpp<ROR8>(c); const float uv = rbf(xrow_sum(c));
            const float inner = 0.7978845608028654f * (gv + 0.044715f * gv * gv * gv);
            const float th2 = 1.0f - 2.0f / (1.0f + __expf(2.0f * inner));
            const float hv = rbf(0.5f * gv * (1.0f + th2)) * uv;
            if (lane < 8) *(LAS unsigned short*)(L + L_O + 160 + ((((4 * lane + e) & 7) * 4) + (lane >> 1)) * 2) = (unsigned short)f2bf(hv);
        }
        asm volatile("s_waitcnt lgkmcnt(0)" ::: "memory");
        if (lane == 0) *hready = 1u;
        STAMP(23);
        PRIO(2);
        krest();
    } else {
        PRIO(2);
        krest();
        STAMP(23);
    }
    for (unsigned sp = 0; *qready == 0u && sp < LDS_SPIN_CAP; ++sp) __builtin_amdgcn_s_sleep(1);
    REPOLL();
    v4u qf[2];
#pragma unroll
    for (int s = 0; s < 2; ++s) qf[s] = *(LAS v4u*)(L + L_Q + (c16 & 7) * 256 + (64 * s + 32 * (c16 >> 3) + 8 * gg) * 2);
    STAMP(4);
    SB();

    auto scores8 = [&](const v4u* kt, float* sc8) {
        f32x4 dU[4];
#pragma unroll
        for (int U = 0; U < 4; ++U) {
            dU[U] = mfma32(kt[2 * U], qf[0], f32x4{0.f, 0.f, 0.f, 0.f});
            dU[U] = mfma32(kt[2 * U + 1], qf[1], dU[U]);
        }
#pragma unroll
        for (int h = 0; h < 2; ++h)
#pragma unroll
            for (int i = 0; i < 4; ++i) {
                auto sw = __builtin_amdgcn_permlane32_swap(fbits(dU[h][i]), fbits(dU[h + 2][i]), false, false);
                const float s = bitsf(sw[0]) + dpp<ROR8>(bitsf(sw[1]));
                sc8[4 * h + i] = rbf(rbf(s) * 0.08838834764831845f);
            }
    };
    f32x4 oa[8];
    auto pv8 = [&](const v4u* vt, v4u pa) {
#pragma unroll
        for (int d = 0; d < 4; ++d) {
            v4u lo, hi;
#pragma unroll
            for (int i = 0; i < 4; ++i) {
                lo[i] = __builtin_amdgcn_perm(vt[2 * i + 1][d], vt[2 * i][d], 0x05040100u);
                hi[i] = __builtin_amdgcn_perm(vt[2 * i + 1][d], vt[2 * i][d], 0x07060302u);
            }
            oa[2 * d] = mfma32(pa, lo, oa[2 * d]);
            oa[2 * d + 1] = mfma32(pa, hi, oa[2 * d + 1]);
        }
    };
    float sc[8 * NSTAT];
#pragma unroll
    for (int t = 0; t < NSTAT; ++t) scores8(&kb[8 * t], &sc[8 * t]);
    STAMP(5);
    float mx = sc[0];
#pragma unroll
    for (int i = 1; i < 8 * NSTAT; ++i) mx = fmaxf(mx, sc[i]);
    mx = xrow_max(mx);
    float lsum = 0.f;
    v4u pf[NSTAT];
    {
        const float mneg = -mx * LOG2E;
#pragma unroll
        for (int t = 0; t < NSTAT; ++t)
#pragma unroll
            for (int d = 0; d < 4; ++d) {
                const float p0 = __builtin_amdgcn_exp2f(fmaf(sc[8 * t + 2 * d], LOG2E, mneg)), p1 = __builtin_amdgcn_exp2f(fmaf(sc[8 * t + 2 * d + 1], LOG2E, mneg));
                lsum += p0 + p1;
                pf[t][d] = pk2(p0, p1);
            }
    }
    auto gemv32 = [&](const v4u (&wv)[4], unsigned ldsoff, int half) {
        const v2u av = *(LAS v2u*)(L + ldsoff + (2 * gg + (lane & 1)) * 8);
        f32x4 acc[8];
        mfma_rows4(wv, av, acc);
        float y[8];
#pragma unroll
        for (int e = 0; e < 8; ++e) y[e] = acc[e][0] + dpp<ROR8>(acc[e][1]);
        const bool up = gg & 1;
        st_sc1(v4u{nosent(up ? y[4] : y[0]), nosent(up ? y[5] : y[1]), nosent(up ? y[6] : y[2]), nosent(up ? y[7] : y[3])},
               rWS, (lane < 32 && c16 < 8) ? obase + OB_AF + (16 * w + 2 * c16 + gg) * 32 + half * 16 : OOB);
    };
    PRIO(1);
#pragma unroll
    for (int j = VE; j < 8 * NSTAT; ++j) vb[j] = vload(j);
    SB();
    STAMP(6);
    for (unsigned sp = 0; *hready == 0u && sp < LDS_SPIN_CAP; ++sp) __builtin_amdgcn_s_sleep(1);
    REPOLL();
    PRIO(3);
    const void* cnt = (const unsigned char*)p.ws + WS_CNT + pool * 128;
    const unsigned cl = claim_issue(cnt);
    SB();
    PRIO(1);

#pragma unroll
    for (int e = 0; e < 8; ++e) oa[e] = f32x4{0.f, 0.f, 0.f, 0.f};
#pragma unroll
    for (int t = 0; t < NSTAT; ++t) { pv8(&vb[8 * t], pf[t]); SB(); }
    STAMP(16);
    PRIO(0);
    unsigned n = claim_wait<0>(cl);
    if (n >= (unsigned)NDYN) { if (lane == 0) __hip_atomic_fetch_add((LAS unsigned*)vcount, 1u, __ATOMIC_RELAXED, __HIP_MEMORY_SCOPE_WORKGROUP); }
    auto tile_loads = [&](unsigned nt, v4u (&kt)[4], v4u (&vt)[4]) {
        const int tb = g * (SK * 256) + (512 * NSTAT * 32 + ((pool & 3) * NDYN + (int)nt) * 16) * 256;
#pragma unroll
        for (int j = 0; j < 4; ++j) kt[j] = ld_nt(rK, kvo, tb + (j >> 1) * 512 + (j & 1) * 128);
#pragma unroll
        for (int j = 0; j < 4; ++j) vt[j] = ld_nt(rV, lane * 16, tb + j * 1024);
        SB();
    };
    auto tile_math = [&](const v4u (&kt)[4], const v4u (&vt)[4]) {
        float s4[4];
        {
            f32x4 d0 = mfma32(kt[0], qf[0], f32x4{0.f, 0.f, 0.f, 0.f}), d1 = mfma32(kt[2], qf[0], f32x4{0.f, 0.f, 0.f, 0.f});
            d0 = mfma32(kt[1], qf[1], d0); d1 = mfma32(kt[3], qf[1], d1);
#pragma unroll
            for (int i = 0; i < 4; ++i) {
                auto sw = __builtin_amdgcn_permlane32_swap(fbits(d0[i]), fbits(d1[i]), false, false);
                const float s = bitsf(sw[0]) + dpp<ROR8>(bitsf(sw[1]));
                s4[i] = rbf(rbf(s) * 0.08838834764831845f);
            }
        }
        const float tm = fmaxf(mx, xrow_max(fmaxf(fmaxf(s4[0], s4[1]), fmaxf(s4[2], s4[3]))));
        if (__any(tm > mx)) {
            const float al = __builtin_amdgcn_exp2f((mx - tm) * LOG2E);
            lsum *= al;
            const float ar[4] = {headbc<0>(al), headbc<1>(al), headbc<2>(al), headbc<3>(al)};
#pragma unroll
            for (int i = 0; i < 4; ++i)
#pragma unroll
                for (int e = 0; e < 8; ++e) oa[e][i] *= ar[i];
            mx = tm;
        }
        const float mneg = -mx * LOG2E;
        float pe[4];
#pragma unroll
        for (int i = 0; i < 4; ++i) { pe[i] = __builtin_amdgcn_exp2f(fmaf(s4[i], LOG2E, mneg)); lsum += pe[i]; }
        const v2u pa = {pk2(pe[0], pe[1]), pk2(pe[2], pe[3])};
#pragma unroll
        for (int d = 0; d < 4; ++d) {
            v2u lo, hi;
            lo.x = __builtin_amdgcn_perm(vt[1][d], vt[0][d], 0x05040100u); lo.y = __builtin_amdgcn_perm(vt[3][d], vt[2][d], 0x05040100u);
            hi.x = __builtin_amdgcn_perm(vt[1][d], vt[0][d], 0x07060302u); hi.y = __builtin_amdgcn_perm(vt[3][d], vt[2][d], 0x07060302u);
            oa[2 * d] = mfma16(pa, lo, oa[2 * d]);
            oa[2 * d + 1] = mfma16(pa, hi, oa[2 * d + 1]);
        }
        RETRY(20);
    };
    auto no_more = [&]() { if (lane == 0) __hip_atomic_fetch_add((LAS unsigned*)vcount, 1u, __ATOMIC_RELAXED, __HIP_MEMORY_SCOPE_WORKGROUP); };
    for (int guard = 0; n < (unsigned)(NDYN - LATE) && guard < NDYN; ++guard) {
        const unsigned c2 = claim_issue(cnt);
        SB();
        v4u kt[4], vt[4];
        tile_loads(n, kt, vt);
        n = claim_wait<8>(c2);
        if (n >= (unsigned)NDYN) no_more();
        tile_math(kt, vt);
    }
    if (LATE > 0) {
        for (int guard = 0; n < (unsigned)NDYN && guard < NDYN; ++guard) {
            v4u kt[4], vt[4];
            tile_loads(n, kt, vt);
            tile_math(kt, vt);
            n = claim_wait<0>(claim_issue(cnt));
            if (n >= (unsigned)NDYN) no_more();
        }
    }
    lsum = xrow_sum(lsum);
    STAMP(8);
    if (lane < 32) {
#pragma unroll
        for (int i = 0; i < 4; ++i) {
            LAS f32x4* m = (LAS f32x4*)(L + L_MRG + w * 4096 + (4 * gg + i) * 512 + c16 * 32);
            m[0] = f32x4{oa[0][i], oa[1][i], oa[2][i], oa[3][i]}; m[1] = f32x4{oa[4][i], oa[5][i], oa[6][i], oa[7][i]};
        }
    }
    if (lane < 8) { LAS float* ml = (LAS float*)(L + L_ML + w * 64 + lane * 8); ml[0] = mx; ml[1] = lsum; }
    const int ocol = 128 * xl + 4 * ii;
    auto fill_requests = [&]() {
        if (!(EARLYFILL & 1)) wd_requests();
        if (!(EARLYFILL & 2)) wo_requests();
        SB(); };
    unsigned arr = 0;
    if (lane == 0) arr = __hip_atomic_fetch_add((LAS unsigned*)(L + L_O + 148), 1u, __ATOMIC_RELAXED, __HIP_MEMORY_SCOPE_WORKGROUP);
    arr = __builtin_amdgcn_readfirstlane(arr);
    const bool early = arr < (unsigned)(NW - NLATE), server = arr == 0u;
    if (early) {
        for (unsigned sp = 0; *vcount < (unsigned)NW && sp < LDS_SPIN_CAP; ++sp) __builtin_amdgcn_s_sleep(1);
        REPOLL();
        fill_requests();
    }
    WG_BARRIER();
    PRIO(3);
    STAMP(7);
    {
        const int hw = (lane >> 5) * 4;
        float mw[8], lw[4]; f32x4 ow[4];
#pragma unroll
        for (int ww = 0; ww < 8; ++ww) mw[ww] = *(LAS float*)(L + L_ML + ww * 64 + w * 8);
#pragma unroll
        for (int i = 0; i < 4; ++i) { lw[i] = *(LAS float*)(L + L_ML + (hw + i) * 64 + w * 8 + 4); ow[i] = *(LAS f32x4*)(L + L_MRG + (hw + i) * 4096 + w * 512 + (lane & 31) * 16); }
        SB();
        const float M = fmaxf(fmaxf(fmaxf(mw[0], mw[1]), fmaxf(mw[2], mw[3])), fmaxf(fmaxf(mw[4], mw[5]), fmaxf(mw[6], mw[7])));
        float Ls = 0.f; f32x4 o = {0.f, 0.f, 0.f, 0.f};
#pragma unroll
        for (int i = 0; i < 4; ++i) {
            const float f = __builtin_amdgcn_exp2f(((lane < 32 ? mw[i] : mw[4 + i]) - M) * LOG2E);
            Ls = fmaf(f, lw[i], Ls);
            o += f * ow[i];
        }
        float o4[5] = {o[0], o[1], o[2], o[3], Ls};
#pragma unroll
        for (int e = 0; e < 5; ++e) { auto t = __builtin_amdgcn_permlane32_swap(fbits(o4[e]), fbits(o4[e]), false, false); o4[e] = bitsf(t[0]) + bitsf(t[1]); }
        st_sc1(v4u{nosent(o4[0]), nosent(o4[1]), nosent(o4[2]), nosent(o4[3])}, rWS, lane < 32 ? obase + OB_A1 + w * 512 + lane * 16 : OOB);
        st_sc1_64(v2u{nosent(M), nosent(o4[4])}, rWS, lane == 32 ? WS_ML + (8 * g + w) * 512 + r * 8 : OOB);
    }
    STAMP(9);
    volatile LAS unsigned* oready = (volatile LAS unsigned*)(L + L_O + 140);
    if (!early) { if (FILLDLY) __builtin_amdgcn_s_sleep(FILLDLY); fill_requests(); gemv32(wd, L_O + 160, 1); }
    else if (!server) gemv32(wd, L_O + 160, 1);
    else {
        v4u a[8]; v2u ml;
        const int gb = WS_OUT + 64 * g * OB_STRIDE + OB_A1 + kk * 128;
        const int avo = (lane >> 3) * OB_STRIDE + (lane & 7) * 16;
        ml = ld_sc1_64(rWS, lane * 8, WS_ML + (8 * g + (kk >> 2)) * 512);
#pragma unroll
        for (int t = 0; t < 8; ++t) a[t] = ld_sc1(rWS, avo, gb + 8 * t * OB_STRIDE);
        SB();
        gemv32(wd, L_O + 160, 1);
        STAMP(3);
        SB();
        auto missing = [&]() { unsigned ms = __any(umax2(ml.x, ml.y) == SENT) ? 256u : 0u;
#pragma unroll
            for (int t = 0; t < 8; ++t) ms |= __any(umax4(a[t]) == SENT) ? 1u << t : 0u;
            return ms; };
        unsigned miss = missing();
        for (unsigned spins = 0; miss != 0u && spins < SPIN_CAP; ++spins) {
            REPOLL(); RETRY(18);
            if (miss & 256u) ml = ld_sc1_64(rWS, lane * 8, WS_ML + (8 * g + (kk >> 2)) * 512);
#pragma unroll
            for (int t = 0; t < 8; ++t) if (miss & (1u << t)) a[t] = ld_sc1(rWS, avo, gb + 8 * t * OB_STRIDE);
            SB();
            miss = missing();
        }
        const float m = bitsf(ml.x), M = wave_max(m), wt = __builtin_amdgcn_exp2f((m - M) * LOG2E);
        const float Ls = wave_sum(wt * bitsf(ml.y));
        float o[4] = {0.f, 0.f, 0.f, 0.f};
#pragma unroll
        for (int t = 0; t < 8; ++t) {
            const float wv = bperm(8 * t + (lane >> 3), wt);
#pragma unroll
            for (int e = 0; e < 4; ++e) o[e] = fmaf(wv, bitsf(a[t][e]), o[e]);
        }
#pragma unroll
        for (int e = 0; e < 4; ++e) { float v = o[e]; v += dpp<ROR8>(v); o[e] = xrow_sum(v) / Ls; }
        if (lane < 8) {
#pragma unroll
            for (int e = 0; e < 4; ++e) *(LAS unsigned short*)(L + L_O + ((((4 * lane + e) & 7) * 4) + (lane >> 1)) * 2) = (unsigned short)f2bf(o[e]);
        }
        asm volatile("s_waitcnt lgkmcnt(0)" ::: "memory");
        if (lane == 0) *oready = 1u;
    }
    for (unsigned sp = 0; *oready == 0u && sp < LDS_SPIN_CAP; ++sp) __builtin_amdgcn_s_sleep(1);
    REPOLL();
    STAMP(10);
    PRIO(3);
    gemv32(wo, L_O, 0);
    STAMP(11);
    if (server) {
        v4u f[4];
        const int onn = ocol >> 9;
        const int fvo = (2 * (lane >> 1) + onn) * OB_STRIDE + (lane & 1) * 16, fb = WS_OUT + OB_AF + ((ocol & 511) >> 2) * 32;
        const v2u xo = __builtin_amdgcn_raw_buffer_load_b64(rX, ocol * 2, 0, 0);
#pragma unroll
        for (int t = 0; t < 4; ++t) f[t] = ld_sc1(rWS, fvo, fb + 64 * t * OB_STRIDE);
        SB();
        auto missing = [&]() { unsigned ms = 0u;
#pragma unroll
            for (int t = 0; t < 4; ++t) ms |= __any(umax4(f[t]) == SENT) ? 1u << t : 0u;
            return ms; };
        unsigned miss = missing();
        for (unsigned spins = 0; miss != 0u && spins < SPIN_CAP; ++spins) {
            REPOLL(); RETRY(19);
#pragma unroll
            for (int t = 0; t < 4; ++t) if (miss & (1u << t)) f[t] = ld_sc1(rWS, fvo, fb + 64 * t * OB_STRIDE);
            SB();
            miss = missing();
        }
        float y[4], gt[4];
#pragma unroll
        for (int e = 0; e < 4; ++e) {
            float v = (bitsf(f[0][e]) + bitsf(f[1][e])) + (bitsf(f[2][e]) + bitsf(f[3][e]));
            v += dpp<ROR2>(v); v += dpp<ROR4>(v); v += dpp<ROR8>(v); y[e] = rbf(xrow_sum(v));
            const float sg = rbf(y[e] / (1.0f + __expf(-y[e])));
            gt[e] = dpp<ROR1>(sg);
        }
        if (lane == 0) {
            const float xv[4] = {bflo(xo.x), bfhi(xo.x), bflo(xo.y), bfhi(xo.y)};
            float o[4];
#pragma unroll
            for (int e = 0; e < 4; ++e) o[e] = xv[e] + rbf(y[e] * gt[e]);
            *(GAS v2u*)((unsigned char*)p.out + ocol * 2) = v2u{pk2(o[0], o[1]), pk2(o[2], o[3])};
            if (b == 0) __hip_atomic_store((gu64*)p.ws, E1, __ATOMIC_RELAXED, __HIP_MEMORY_SCOPE_AGENT);
        }
    }
    STAMP(12);
}
}

extern "C" void kernel_launch(void* const* d_in, const int* in_sizes, int n_in, void* d_out, int out_size, void* d_ws, size_t ws_size, hipStream_t stream) {
    (void)in_sizes; (void)n_in; (void)out_size; (void)ws_size;
    Params p;
    p.x = d_in[0]; p.kc = d_in[1]; p.vc = d_in[2]; p.wq = d_in[3]; p.wo = d_in[4];
    p.wg = d_in[6]; p.wu = d_in[7]; p.wd = d_in[8];
    p.out = d_out; p.ws = d_ws;
    (void)hipFuncSetAttribute((const void*)fused_decode, hipFuncAttributeMaxDynamicSharedMemorySize, LDS_BYTES);
    hipLaunchKernelGGL(fused_decode, dim3(NCU), dim3(NTHR), LDS_BYTES, stream, p);
}
```

```cpp
#include <hip/hip_runtime.h>
#include <hip/hip_bf16.h>
#include <stdint.h>

#define DIAG 0

namespace {
#define GAS __attribute__((address_space(1)))
#define LAS __attribute__((address_space(3)))
typedef unsigned v4u __attribute__((ext_vector_type(4)));
typedef unsigned v2u __attribute__((ext_vector_type(2)));
typedef float f32x4 __attribute__((ext_vector_type(4)));
typedef short bf16x8 __attribute__((ext_vector_type(8)));
typedef short bf16x4 __attribute__((ext_vector_type(4)));
typedef __amdgpu_buffer_rsrc_t rsrc_t;
typedef GAS unsigned long long gu64;

constexpr int DM = 1024, SK = 65536, DF = 3584;
constexpr int NCU = 256, NW = 8, NTHR = NW * 64;
constexpr int AUX_NT = 2, AUX_SC1 = 16;
constexpr int RSRC_W3 = 0x00020000;
constexpr unsigned SENT = 0xFFFFFFFFu;
constexpr unsigned SPIN_CAP = 1u << 16;

constexpr unsigned WS_EPOCH = 0;
constexpr unsigned WS_FLAG = 256;
constexpr unsigned WS_QG = WS_FLAG + 2048;
constexpr unsigned WS_QP = WS_QG + 32768;
constexpr unsigned WS_ML = WS_QP + 256 * 1024;
constexpr unsigned WS_OUT = WS_ML + 32 * 512;
constexpr unsigned OB_A1 = 0, OB_AF = 4096, OB_F1 = OB_AF + 4096, OB_BYTES = OB_F1 + 1792, OB_STRIDE = 10112;
constexpr unsigned WS_END = WS_OUT + NCU * OB_STRIDE;
constexpr unsigned WS_DIAG = 8u << 20;
static_assert(WS_OUT % 128 == 0 && OB_BYTES <= OB_STRIDE && OB_STRIDE % 128 == 0 && WS_END < WS_DIAG, "ws map");

constexpr unsigned L_RED = 0;
constexpr unsigned L_Q = 2048;
constexpr unsigned L_F1 = L_Q + 2048;
constexpr unsigned L_O = L_F1 + 1024;
constexpr unsigned L_ML = L_O + 128;
constexpr unsigned L_GU = L_ML + 512;
constexpr unsigned L_MRG = L_GU + 8 * 448 * 4;
constexpr unsigned L_END = L_MRG + 32768;
constexpr unsigned LDS_BYTES = 96 * 1024;
static_assert(L_END <= LDS_BYTES && L_GU % 16 == 0 && L_MRG % 16 == 0, "lds map");

__device__ __forceinline__ float bf2f(unsigned h) { return __builtin_bit_cast(float, h << 16); }
__device__ __forceinline__ float bflo(unsigned u) { return __builtin_bit_cast(float, u << 16); }
__device__ __forceinline__ float bfhi(unsigned u) { return __builtin_bit_cast(float, u & 0xffff0000u); }
__device__ __forceinline__ unsigned f2bf(float f) { return (unsigned)__builtin_bit_cast(unsigned short, (__bf16)f); }
__device__ __forceinline__ float rbf(float f) { return bf2f(f2bf(f)); }
__device__ __forceinline__ unsigned pk2(float lo, float hi) {
    typedef __bf16 b2 __attribute__((ext_vector_type(2))); typedef float f2 __attribute__((ext_vector_type(2)));
    return __builtin_bit_cast(unsigned, __builtin_convertvector((f2){lo, hi}, b2)); }
__device__ __forceinline__ unsigned fbits(float f) { return __builtin_bit_cast(unsigned, f); }
__device__ __forceinline__ float bitsf(unsigned u) { return __builtin_bit_cast(float, u); }
__device__ __forceinline__ unsigned nosent(float f) { const unsigned u = fbits(f); return u < 0xFFFFFFFEu ? u : 0xFFFFFFFEu; }
template <int CTRL> __device__ __forceinline__ float dpp(float x) { return bitsf((unsigned)__builtin_amdgcn_mov_dpp((int)fbits(x), CTRL, 0xf, 0xf, true)); }
constexpr int ROR1 = 0x121, ROR2 = 0x122, ROR4 = 0x124, ROR8 = 0x128;
__device__ __forceinline__ float xrow_sum(float x) {
    auto s = __builtin_amdgcn_permlane16_swap(fbits(x), fbits(x), false, false); x = bitsf(s[0]) + bitsf(s[1]);
    auto t = __builtin_amdgcn_permlane32_swap(fbits(x), fbits(x), false, false); return bitsf(t[0]) + bitsf(t[1]); }
__device__ __forceinline__ float xrow_max(float x) {
    auto s = __builtin_amdgcn_permlane16_swap(fbits(x), fbits(x), false, false); x = fmaxf(bitsf(s[0]), bitsf(s[1]));
    auto t = __builtin_amdgcn_permlane32_swap(fbits(x), fbits(x), false, false); return fmaxf(bitsf(t[0]), bitsf(t[1])); }
__device__ __forceinline__ float wave_sum(float x) { x += dpp<ROR1>(x); x += dpp<ROR2>(x); x += dpp<ROR4>(x); x += dpp<ROR8>(x); return xrow_sum(x); }
__device__ __forceinline__ float wave_max(float x) { x = fmaxf(x, dpp<ROR1>(x)); x = fmaxf(x, dpp<ROR2>(x)); x = fmaxf(x, dpp<ROR4>(x)); x = fmaxf(x, dpp<ROR8>(x)); return xrow_max(x); }
__device__ __forceinline__ rsrc_t mk_rsrc(const void* p, unsigned bytes) { return __builtin_amdgcn_make_buffer_rsrc((void*)p, 0, (int)bytes, RSRC_W3); }
__device__ __forceinline__ v4u ld_nt(rsrc_t r, int voff, int soff) { return __builtin_amdgcn_raw_buffer_load_b128(r, voff, soff, AUX_NT); }
__device__ __forceinline__ v4u ld_sc1(rsrc_t r, int voff, int soff) { return __builtin_amdgcn_raw_buffer_load_b128(r, voff, soff, AUX_SC1); }
__device__ __forceinline__ v2u ld_sc1_64(rsrc_t r, int voff, int soff) { return __builtin_amdgcn_raw_buffer_load_b64(r, voff, soff, AUX_SC1); }
__device__ __forceinline__ void st_sc1(v4u v, rsrc_t r, int voff, int base) { __builtin_amdgcn_raw_buffer_store_b128(v, r, voff + base, 0, AUX_SC1); }
__device__ __forceinline__ unsigned umax4(v4u v) { const unsigned a = v.x > v.y ? v.x : v.y, b = v.z > v.w ? v.z : v.w; return a > b ? a : b; }
__device__ __forceinline__ unsigned umax2(unsigned a, unsigned b) { return a > b ? a : b; }
#define PIN8(a) asm volatile("" : "+v"(a[0]), "+v"(a[1]), "+v"(a[2]), "+v"(a[3]), "+v"(a[4]), "+v"(a[5]), "+v"(a[6]), "+v"(a[7]))
__device__ __forceinline__ f32x4 mfma32(v4u a, v4u b, f32x4 c) { return __builtin_amdgcn_mfma_f32_16x16x32_bf16(__builtin_bit_cast(bf16x8, a), __builtin_bit_cast(bf16x8, b), c, 0, 0, 0); }
__device__ __forceinline__ f32x4 mfma16(v2u a, v2u b, f32x4 c) { return __builtin_amdgcn_mfma_f32_16x16x16bf16_1k(__builtin_bit_cast(bf16x4, a), __builtin_bit_cast(bf16x4, b), c, 0, 0, 0); }
#define SB() __builtin_amdgcn_sched_barrier(0)
#define XBAR 1
#define CLR_AT -1
#define PUBBAR 1
#define PACE_B 270
#define PACE_A 180
#define PACE_WD 100
#define PACE(k) do { if (PACE_B > 0) { while (__builtin_amdgcn_s_memrealtime() - T0 < (unsigned long long)(PACE_A + (k) * PACE_B + ((k) >= 6 ? PACE_WD : 0))) __builtin_amdgcn_s_sleep(2); SB(); } } while (0)
#define REPOLL() asm volatile("" ::: "memory")
#define WG_BARRIER() asm volatile("s_waitcnt lgkmcnt(0)\n\ts_barrier" ::: "memory")
constexpr int OOB = (int)0x80000000u;
constexpr float LOG2E = 1.4426950408889634f;

#define QP_AT 6
#define QG_AT 16
#define QC_AT 24
#define FLG_AT 2
#define RETRY(k) do { } while (0)
#define STAMP(k) do { } while (0)

struct Params {
    const void* x; const void* kc; const void* vc; const void* wq; const void* wo; const void* wg; const void* wu; const void* wd;
    void* out; void* ws;
};

__device__ __forceinline__ void mfma_rows4(const v4u (&wv)[4], v2u a, f32x4 (&acc)[8]) {
#pragma unroll
    for (int d = 0; d < 4; ++d) {
        v2u lo, hi;
        lo.x = __builtin_amdgcn_perm(wv[1][d], wv[0][d], 0x05040100u); lo.y = __builtin_amdgcn_perm(wv[3][d], wv[2][d], 0x05040100u);
        hi.x = __builtin_amdgcn_perm(wv[1][d], wv[0][d], 0x07060302u); hi.y = __builtin_amdgcn_perm(wv[3][d], wv[2][d], 0x07060302u);
        acc[2 * d] = mfma16(a, lo, f32x4{0.f, 0.f, 0.f, 0.f});
        acc[2 * d + 1] = mfma16(a, hi, f32x4{0.f, 0.f, 0.f, 0.f});
    }
}

__global__ void __launch_bounds__(NTHR, 2) fused_decode(Params p) {
    extern __shared__ __attribute__((aligned(16))) unsigned char smem[];
    LAS unsigned char* L = (LAS unsigned char*)smem;
    const int tid = threadIdx.x, lane = tid & 63, w = __builtin_amdgcn_readfirstlane(tid >> 6);
    const int b = blockIdx.x, xl = b & 7, ii = b >> 3, g = ii >> 3, r = 8 * xl + (ii & 7), cid = 64 * g + r;
    const int gg = lane >> 4, c16 = lane & 15;
    STAMP(0);
    const unsigned long long T0 = __builtin_amdgcn_s_memrealtime();
    const rsrc_t rX = mk_rsrc(p.x, DM * 2), rK = mk_rsrc(p.kc, 4u * SK * 256), rV = mk_rsrc(p.vc, 4u * SK * 256);
    const rsrc_t rWq = mk_rsrc(p.wq, DM * 4096 * 2), rWo = mk_rsrc(p.wo, 4096 * DM * 2), rWd = mk_rsrc(p.wd, DF * DM * 2);
    const rsrc_t rWS = mk_rsrc(p.ws, WS_END);
    const int ks = cid >> 4, cg = cid & 15;
    const rsrc_t rGU = mk_rsrc((cg >> 3) ? p.wu : p.wg, DM * DF * 2);
    const int obase = WS_OUT + cid * OB_STRIDE;

    const unsigned long long E = __hip_atomic_load((gu64*)p.ws, __ATOMIC_RELAXED, __HIP_MEMORY_SCOPE_AGENT);
    const int tj = ii & 3;
    const v4u xa = __builtin_amdgcn_raw_buffer_load_b128(rX, lane * 32, 0, 0), xb = __builtin_amdgcn_raw_buffer_load_b128(rX, lane * 32 + 16, 0, 0);
    unsigned xq[4];
#pragma unroll
    for (int t = 0; t < 4; ++t) xq[t] = __builtin_amdgcn_raw_buffer_load_b16(rX, (256 * tj + 64 * t + 8 * w + (lane >> 3)) * 2, 0, 0);
    const unsigned xg = __builtin_amdgcn_raw_buffer_load_b16(rX, (64 * ks + 8 * w + (lane & 7)) * 2, 0, 0);
    SB();
    if (XBAR) asm volatile("s_barrier" ::: "memory");
    SB();
    v4u wq[4];
    {
        const int vo = (256 * tj + 8 * w + (lane >> 3)) * 8192 + (16 * (cid - tj)) * 2 + (lane & 7) * 16;
#pragma unroll
        for (int t = 0; t < 4; ++t) wq[t] = ld_nt(rWq, vo, t * 64 * 8192);
    }
    SB();
    auto clears = [&]() {
        const v4u s4 = {SENT, SENT, SENT, SENT};
        st_sc1(s4, rWS, tid * 16, obase);
        st_sc1(s4, rWS, tid < (int)(OB_BYTES / 16 - NTHR) ? (NTHR + tid) * 16 : OOB, obase);
        __builtin_amdgcn_raw_buffer_store_b64(v2u{SENT, SENT}, rWS, tid >= NTHR - 8 ? WS_ML + (8 * g + tid - (NTHR - 8)) * 512 + r * 8 : OOB, 0, AUX_SC1);
        SB(); };
    if (CLR_AT < 0) clears();
    const unsigned long long tag48 = (E + 1) & 0xFFFFFFFFFFFFull;
    const unsigned th = (unsigned)(tag48 >> 32), tl = (unsigned)tag48;
    auto badtag = [&](v4u q) { return (((q.x ^ tl) | (q.z ^ tl)) | (((q.y ^ th) | (q.w ^ th)) & 0xffffu)) != 0u; };

    float mean_b, sd_b;
    {
        float xv[16];
#pragma unroll
        for (int d = 0; d < 4; ++d) { xv[2 * d] = bflo(xa[d]); xv[2 * d + 1] = bfhi(xa[d]); xv[8 + 2 * d] = bflo(xb[d]); xv[8 + 2 * d + 1] = bfhi(xb[d]); }
        float s = 0.f;
#pragma unroll
        for (int d = 0; d < 16; ++d) s += xv[d];
        const float mean = wave_sum(s) * (1.0f / DM);
        float v = 0.f;
#pragma unroll
        for (int d = 0; d < 16; ++d) { const float t = xv[d] - mean; v = fmaf(t, t, v); }
        const float var = rbf(wave_sum(v) * (1.0f / DM));
        mean_b = rbf(mean);
        sd_b = rbf(sqrtf(rbf(var + 1e-5f)));
        asm volatile("" : "+v"(mean_b), "+v"(sd_b));
    }
    auto norm = [&](unsigned xbits) -> float { return rbf(rbf(bf2f(xbits) - mean_b) / sd_b); };
    STAMP(1);
    SB();
    v4u gu[8];
    {
        const int vo = lane < 56 ? (448 * (cg & 7) + 8 * lane) * 2 : OOB;
        const int so = (64 * ks + 8 * w) * (DF * 2);
#pragma unroll
        for (int j = 0; j < 8; ++j) { if (j == CLR_AT) { SB(); clears(); } gu[j] = ld_nt(rGU, vo, so + j * (DF * 2)); }
    }
    SB();

    {
        float acc[8];
#pragma unroll
        for (int e = 0; e < 8; ++e) acc[e] = 0.f;
#pragma unroll
        for (int t = 0; t < 4; ++t) { const float xn = norm(xq[t]);
#pragma unroll
            for (int d = 0; d < 4; ++d) { acc[2 * d] = fmaf(xn, bflo(wq[t][d]), acc[2 * d]); acc[2 * d + 1] = fmaf(xn, bfhi(wq[t][d]), acc[2 * d + 1]); } }
#pragma unroll
        for (int e = 0; e < 8; ++e) { float v = acc[e]; v += dpp<ROR8>(v); acc[e] = xrow_sum(v); }
        if (lane < 8) {
            LAS f32x4* red = (LAS f32x4*)(L + L_RED + w * 256 + lane * 32);
            red[0] = f32x4{acc[0], acc[1], acc[2], acc[3]}; red[1] = f32x4{acc[4], acc[5], acc[6], acc[7]};
        }
    }
    WG_BARRIER();
    if (w == 0) {
        float q = 0.f;
#pragma unroll
        for (int ww = 0; ww < 8; ++ww) q += ((LAS float*)(L + L_RED))[ww * 64 + lane];
        const unsigned u = fbits(q);
        st_sc1(v4u{tl, th | (u & 0xffff0000u), tl, th | (u << 16)}, rWS, lane * 16, WS_QP + cid * 1024);
    }
    STAMP(2);
    SB();

    const int kvo = (4 * (lane & 3) + ((lane >> 2) & 1)) * 256 + ((lane >> 3) & 1) * 64 + gg * 16;
    const int tbase = g * (SK * 256) + (8 * r + w) * 8192;
    auto kload = [&](int j) -> v4u { const int t = j >> 3, U = (j >> 1) & 3, s = j & 1; return ld_nt(rK, kvo, tbase + t * (512 * 8192) + (16 * (U & 1) + 2 * (U >> 1)) * 256 + s * 128); };
    auto vload = [&](int j) -> v4u { const int t = j >> 3, jj = j & 7; return ld_nt(rV, lane * 16, tbase + t * (512 * 8192) + jj * 1024); };
    v4u kb[32];
    v4u qp = {0u, 0u, 0u, 0u};
    const int qpo = WS_QP + (cid - tj + gg) * 1024 + (16 * tj + c16) * 16;
    {
        const float xgn = norm(xg);
        float acc[8];
#pragma unroll
        for (int e = 0; e < 8; ++e) acc[e] = 0.f;
#pragma unroll
        for (int j = 0; j < 8; ++j) {
            const float xn = bitsf(__builtin_amdgcn_readlane(fbits(xgn), j));
#pragma unroll
            for (int d = 0; d < 4; ++d) { acc[2 * d] = fmaf(xn, bflo(gu[j][d]), acc[2 * d]); acc[2 * d + 1] = fmaf(xn, bfhi(gu[j][d]), acc[2 * d + 1]); }
            PIN8(acc);
            SB();
            if (j == QP_AT && w == 0) qp = ld_sc1(rWS, qpo, 0);
            kb[j] = kload(j);
            SB();
        }
        if (lane < 56) {
            LAS f32x4* gr = (LAS f32x4*)(L + L_GU + w * 1792 + lane * 32);
            gr[0] = f32x4{acc[0], acc[1], acc[2], acc[3]}; gr[1] = f32x4{acc[4], acc[5], acc[6], acc[7]};
        }
    }
    WG_BARRIER();
    if (tid < 112) {
        f32x4 s = {0.f, 0.f, 0.f, 0.f};
#pragma unroll
        for (int ww = 0; ww < 8; ++ww) s += *(LAS f32x4*)(L + L_GU + ww * 1792 + tid * 16);
        st_sc1(v4u{nosent(s[0]), nosent(s[1]), nosent(s[2]), nosent(s[3])}, rWS, tid * 16, obase + OB_F1);
    }
    if (tid == 448) __hip_atomic_store((gu64*)((unsigned char*)p.ws + WS_FLAG) + cid, E + 1, __ATOMIC_RELAXED, __HIP_MEMORY_SCOPE_AGENT);
    STAMP(3);
    if (w == 0) {
        if (__any(badtag(qp))) {
            unsigned spins = 0;
            do { REPOLL(); qp = ld_sc1(rWS, qpo, 0); RETRY(21); } while (__any(badtag(qp)) && ++spins < SPIN_CAP);
        }
        const float q = xrow_sum(bitsf((qp.y & 0xffff0000u) | (qp.w >> 16)));
        if (lane < 16) {
            const unsigned long long gr = ((unsigned long long)f2bf(q) << 48) | tag48;
            __hip_atomic_store((gu64*)((unsigned char*)p.ws + WS_QG) + 16 * cid + lane, gr, __ATOMIC_RELAXED, __HIP_MEMORY_SCOPE_AGENT);
        }
    }
    STAMP(14);
    SB();
    v4u qg;
    PACE(1);
#pragma unroll
    for (int j = 8; j < 24; ++j) {
        if (j == 16) { SB(); PACE(2); }
        if (j == QG_AT) { SB(); qg = ld_sc1(rWS, lane * 16, WS_QG + g * 8192 + w * 1024); SB(); }
        kb[j] = kload(j);
    }
    SB();
    if (QG_AT >= 24) { qg = ld_sc1(rWS, lane * 16, WS_QG + g * 8192 + w * 1024); SB(); }
#pragma unroll
    for (int j = 24; j < QC_AT; ++j) kb[j] = kload(j);
    SB();
    {
        if (__any(badtag(qg))) {
            unsigned spins = 0;
            do { REPOLL(); qg = ld_sc1(rWS, lane * 16, WS_QG + g * 8192 + w * 1024); RETRY(13); } while (__any(badtag(qg)) && ++spins < SPIN_CAP);
        }
        *(LAS unsigned*)(L + L_Q + w * 256 + lane * 4) = (qg.y >> 16) | (qg.w & 0xffff0000u);
    }
    WG_BARRIER();
    v4u qf[2];
#pragma unroll
    for (int s = 0; s < 2; ++s) qf[s] = *(LAS v4u*)(L + L_Q + (c16 & 7) * 256 + (64 * s + 32 * (c16 >> 3) + 8 * gg) * 2);
    STAMP(4);
    SB();

    float sc[32];
    v4u vb[32];
    v4u flg;
#pragma unroll
    for (int t = 0; t < 4; ++t) {
        f32x4 dU[4];
#pragma unroll
        for (int U = 0; U < 4; ++U) {
            dU[U] = mfma32(kb[8 * t + 2 * U], qf[0], f32x4{0.f, 0.f, 0.f, 0.f});
            dU[U] = mfma32(kb[8 * t + 2 * U + 1], qf[1], dU[U]);
        }
        SB();
        if (t == FLG_AT) flg = ld_sc1(rWS, lane < 16 ? lane * 16 : OOB, WS_FLAG + w * 256);
        if (t < 3) PACE(3 + t);
        if (t < 1) {
#pragma unroll
            for (int j = QC_AT - 24; j < 8; ++j) kb[24 + j] = kload(24 + j);
        } else if (t < 3) {
#pragma unroll
            for (int j = 0; j < 8; ++j) vb[8 * (t - 1) + j] = vload(8 * (t - 1) + j);
        }
        SB();
#pragma unroll
        for (int h = 0; h < 2; ++h)
#pragma unroll
            for (int i = 0; i < 4; ++i) {
                auto sw = __builtin_amdgcn_permlane32_swap(fbits(dU[h][i]), fbits(dU[h + 2][i]), false, false);
                const float s = bitsf(sw[0]) + dpp<ROR8>(bitsf(sw[1]));
                sc[8 * t + 4 * h + i] = rbf(rbf(s) * 0.08838834764831845f);
            }
    }
    STAMP(5);
    {
        float mx = sc[0];
#pragma unroll
        for (int i = 1; i < 32; ++i) mx = fmaxf(mx, sc[i]);
        mx = xrow_max(mx);
        if (lane < 8) *(LAS float*)(L + L_ML + w * 32 + lane * 4) = mx;
    }
    {
        const unsigned el = (unsigned)(E + 1), eh = (unsigned)((E + 1) >> 32);
        auto bad = [&](v4u f) { return (lane < 16) & ((((f.x ^ el) | (f.z ^ el)) | ((f.y ^ eh) | (f.w ^ eh))) != 0u); };
        if (__any(bad(flg))) {
            unsigned spins = 0;
            do { REPOLL(); __builtin_amdgcn_s_sleep(2); flg = ld_sc1(rWS, lane < 16 ? lane * 16 : OOB, WS_FLAG + w * 256); } while (__any(bad(flg)) && ++spins < SPIN_CAP);
            RETRY(16);
        }
    }
    WG_BARRIER();
    float Mcu;
    v4u pf[4];
    {
        const LAS float* mp = (const LAS float*)(L + L_ML) + (c16 & 7);
        Mcu = fmaxf(fmaxf(fmaxf(mp[0], mp[8]), fmaxf(mp[16], mp[24])), fmaxf(fmaxf(mp[32], mp[40]), fmaxf(mp[48], mp[56])));
        const float mneg = -Mcu * LOG2E;
        float lsum = 0.f;
#pragma unroll
        for (int t = 0; t < 4; ++t)
#pragma unroll
            for (int d = 0; d < 4; ++d) {
                const float p0 = __builtin_amdgcn_exp2f(fmaf(sc[8 * t + 2 * d], LOG2E, mneg)), p1 = __builtin_amdgcn_exp2f(fmaf(sc[8 * t + 2 * d + 1], LOG2E, mneg));
                lsum += p0 + p1;
                pf[t][d] = pk2(p0, p1);
            }
        lsum = xrow_sum(lsum);
        if (lane < 8) *(LAS float*)(L + L_ML + 256 + w * 32 + lane * 4) = lsum;
    }
    SB();
    const int kkd = cid >> 1, nn = cid & 1, kk = r >> 1;
    const int f1vo = (lane < 32 && (lane & 7) < 7) ? (16 * (4 * (w & 3) + (lane >> 3)) + 8 * (w >> 2) + (kkd >> 4)) * OB_STRIDE + 112 * (kkd & 15) + 16 * (lane & 7) : OOB;
    v4u f1 = ld_sc1(rWS, f1vo, WS_OUT + OB_F1);
    v4u wd[4];
#pragma unroll
    for (int j = 0; j < 4; ++j) { const int row = 8 * j + 2 * gg + ((lane >> 3) & 1); wd[j] = ld_nt(rWd, row < 28 ? (28 * kkd + row) * 2048 + (lane & 7) * 16 : OOB, (512 * nn + 64 * w) * 2); }
    SB();
    STAMP(6);

    f32x4 oa[8];
#pragma unroll
    for (int e = 0; e < 8; ++e) oa[e] = f32x4{0.f, 0.f, 0.f, 0.f};
    v4u wo[4];
    auto pv = [&](int t) {
#pragma unroll
        for (int d = 0; d < 4; ++d) {
            v4u lo, hi;
#pragma unroll
            for (int i = 0; i < 4; ++i) {
                lo[i] = __builtin_amdgcn_perm(vb[8 * t + 2 * i + 1][d], vb[8 * t + 2 * i][d], 0x05040100u);
                hi[i] = __builtin_amdgcn_perm(vb[8 * t + 2 * i + 1][d], vb[8 * t + 2 * i][d], 0x07060302u);
            }
            oa[2 * d] = mfma32(pf[t], lo, oa[2 * d]);
            oa[2 * d + 1] = mfma32(pf[t], hi, oa[2 * d + 1]);
        }
    };
    pv(0);
    SB();
    PACE(6);
#pragma unroll
    for (int j = 16; j < 24; ++j) vb[j] = vload(j);
    SB();
    pv(1);
    SB();
    PACE(7);
#pragma unroll
    for (int j = 24; j < 32; ++j) vb[j] = vload(j);
    SB();
    auto gemv32 = [&](const v4u (&wv)[4], v2u av) -> f32x4 {
        f32x4 acc[8];
        mfma_rows4(wv, av, acc);
        float y[8];
#pragma unroll
        for (int e = 0; e < 8; ++e) y[e] = acc[e][0] + dpp<ROR8>(acc[e][1]);
        const bool up = gg & 1;
        return f32x4{up ? y[4] : y[0], up ? y[5] : y[1], up ? y[6] : y[2], up ? y[7] : y[3]};
    };
    f32x4 fdown;
    {
        auto bad = [&](v4u f) { return umax4(f) == SENT; };
        if (__any(bad(f1))) {
            unsigned spins = 0;
            do { REPOLL(); __builtin_amdgcn_s_sleep(2); f1 = ld_sc1(rWS, f1vo, WS_OUT + OB_F1); RETRY(17); } while (__any(bad(f1)) && ++spins < SPIN_CAP);
        }
        f32x4 a;
#pragma unroll
        for (int e = 0; e < 4; ++e) {
            float t = bitsf(f1[e]); t += dpp<ROR8>(t);
            auto s0 = __builtin_amdgcn_permlane16_swap(fbits(t), fbits(t), false, false); a[e] = bitsf(s0[0]) + bitsf(s0[1]);
        }
        if (lane < 8) *(LAS f32x4*)(L + L_F1 + w * 128 + lane * 16) = a;
    }
    WG_BARRIER();
    {
        unsigned hb[4];
#pragma unroll
        for (int j = 0; j < 4; ++j) {
            const LAS float* f = (const LAS float*)(L + L_F1) + 8 * j + 2 * gg + (lane & 1);
            const float gv = rbf((f[0] + f[32]) + (f[64] + f[96])), uv = rbf((f[128] + f[160]) + (f[192] + f[224]));
            const float inner = 0.7978845608028654f * (gv + 0.044715f * gv * gv * gv);
            const float th = 1.0f - 2.0f / (1.0f + __expf(2.0f * inner));
            hb[j] = f2bf(rbf(0.5f * gv * (1.0f + th)) * uv);
        }
        fdown = gemv32(wd, v2u{hb[0] | (hb[1] << 16), hb[2] | (hb[3] << 16)});
        if (w == 0 && lane < 8) {
            const LAS float* lp = (const LAS float*)(L + L_ML + 256) + lane;
            const float Ls = ((lp[0] + lp[8]) + (lp[16] + lp[24])) + ((lp[32] + lp[40]) + (lp[48] + lp[56]));
            __builtin_amdgcn_raw_buffer_store_b64(v2u{nosent(Mcu), nosent(Ls)}, rWS, WS_ML + (8 * g + lane) * 512 + r * 8, 0, AUX_SC1);
        }
    }
    STAMP(7);
    SB();
    pv(2);
    SB();
#pragma unroll
    for (int j = 0; j < 4; ++j) wo[j] = ld_nt(rWo, (1024 * g + 32 * kk + 8 * j + 2 * gg + ((lane >> 3) & 1)) * 2048 + (lane & 7) * 16, (512 * nn + 64 * w) * 2);
    SB();
    pv(3);
    STAMP(8);

    if (lane < 32) {
#pragma unroll
        for (int i = 0; i < 4; ++i) {
            LAS f32x4* m = (LAS f32x4*)(L + L_MRG + w * 4096 + (4 * gg + i) * 512 + c16 * 32);
            m[0] = f32x4{oa[0][i], oa[1][i], oa[2][i], oa[3][i]}; m[1] = f32x4{oa[4][i], oa[5][i], oa[6][i], oa[7][i]};
        }
    }
    v2u ml;
    if (w == 0) ml = ld_sc1_64(rWS, lane * 8, WS_ML + (8 * g + (kk >> 2)) * 512);
    WG_BARRIER();
    {
        const int hw = (lane >> 5) * 4;
        f32x4 ow[4];
#pragma unroll
        for (int i = 0; i < 4; ++i) ow[i] = *(LAS f32x4*)(L + L_MRG + (hw + i) * 4096 + w * 512 + (lane & 31) * 16);
        const f32x4 o = (ow[0] + ow[1]) + (ow[2] + ow[3]);
        float o4[4];
#pragma unroll
        for (int e = 0; e < 4; ++e) { auto t = __builtin_amdgcn_permlane32_swap(fbits(o[e]), fbits(o[e]), false, false); o4[e] = bitsf(t[0]) + bitsf(t[1]); }
        st_sc1(v4u{nosent(o4[0]), nosent(o4[1]), nosent(o4[2]), nosent(o4[3])}, rWS, lane < 32 ? w * 512 + lane * 16 : OOB, obase + OB_A1);
    }
    STAMP(9);
    if (PUBBAR) asm volatile("s_barrier" ::: "memory");
    const int ocol = 128 * xl + 4 * ii;
    if (w == 0) {
        v4u a[8];
        const int gb = WS_OUT + 64 * g * OB_STRIDE + OB_A1 + kk * 128;
        const int avo = (lane >> 3) * OB_STRIDE + (lane & 7) * 16;
#pragma unroll
        for (int t = 0; t < 8; ++t) a[t] = ld_sc1(rWS, avo, gb + 8 * t * OB_STRIDE);
        SB();
        if (__any(umax2(ml.x, ml.y) == SENT)) {
            unsigned spins = 0;
            do { REPOLL(); ml = ld_sc1_64(rWS, lane * 8, WS_ML + (8 * g + (kk >> 2)) * 512); RETRY(20); } while (__any(umax2(ml.x, ml.y) == SENT) && ++spins < SPIN_CAP);
        }
        const float m = bitsf(ml.x), M = wave_max(m), wt0 = __builtin_amdgcn_exp2f((m - M) * LOG2E);
        const float wt = wt0 / wave_sum(wt0 * bitsf(ml.y));
        float wv[8];
#pragma unroll
        for (int t = 0; t < 8; ++t) wv[t] = bitsf((unsigned)__builtin_amdgcn_ds_bpermute((8 * t + (lane >> 3)) * 4, (int)fbits(wt)));
        SB();
        auto anymiss = [&]() { unsigned m = 0u;
#pragma unroll
            for (int t = 0; t < 8; ++t) m = umax2(m, umax4(a[t]));
            return __any(m == SENT); };
        for (unsigned spins = 0; anymiss() && spins < SPIN_CAP; ++spins) {
            REPOLL(); RETRY(18);
            v4u n[8];
#pragma unroll
            for (int t = 0; t < 8; ++t) n[t] = ld_sc1(rWS, umax4(a[t]) == SENT ? avo : OOB, gb + 8 * t * OB_STRIDE);
            SB();
#pragma unroll
            for (int t = 0; t < 8; ++t) { const bool ms = umax4(a[t]) == SENT; a[t] = ms ? n[t] : a[t]; }
        }
        float o[4] = {0.f, 0.f, 0.f, 0.f};
#pragma unroll
        for (int t = 0; t < 8; ++t)
#pragma unroll
            for (int e = 0; e < 4; ++e) o[e] = fmaf(wv[t], bitsf(a[t][e]), o[e]);
#pragma unroll
        for (int e = 0; e < 4; ++e) { float v = o[e]; v += dpp<ROR8>(v); o[e] = xrow_sum(v); }
        if (lane < 8) {
#pragma unroll
            for (int e = 0; e < 4; ++e) *(LAS unsigned short*)(L + L_O + ((((4 * lane + e) & 7) * 4) + (lane >> 1)) * 2) = (unsigned short)f2bf(o[e]);
        }
    }
    WG_BARRIER();
    STAMP(10);
    {
        const f32x4 ys = gemv32(wo, *(LAS v2u*)(L + L_O + (2 * gg + (lane & 1)) * 8));
        const f32x4 y = gg < 2 ? ys : fdown;
        st_sc1(v4u{nosent(y[0]), nosent(y[1]), nosent(y[2]), nosent(y[3])}, rWS, c16 < 8 ? (16 * w + 2 * c16 + (gg & 1)) * 32 + (gg >> 1) * 16 : OOB, obase + OB_AF);
    }
    STAMP(11);
    if (PUBBAR) asm volatile("s_barrier" ::: "memory");
    if (w == 0) {
        v4u f[4];
        const int onn = ocol >> 9;
        const int fvo = (2 * (lane >> 1) + onn) * OB_STRIDE + (lane & 1) * 16, fb = WS_OUT + OB_AF + ((ocol & 511) >> 2) * 32;
        const v2u xo = __builtin_amdgcn_raw_buffer_load_b64(rX, ocol * 2, 0, 0);
#pragma unroll
        for (int t = 0; t < 4; ++t) f[t] = ld_sc1(rWS, fvo, fb + 64 * t * OB_STRIDE);
        SB();
        auto anymiss = [&]() { return __any(umax2(umax2(umax4(f[0]), umax4(f[1])), umax2(umax4(f[2]), umax4(f[3]))) == SENT); };
        for (unsigned spins = 0; anymiss() && spins < SPIN_CAP; ++spins) {
            REPOLL(); RETRY(19);
            v4u n[4];
#pragma unroll
            for (int t = 0; t < 4; ++t) n[t] = ld_sc1(rWS, umax4(f[t]) == SENT ? fvo : OOB, fb + 64 * t * OB_STRIDE);
            SB();
#pragma unroll
            for (int t = 0; t < 4; ++t) { const bool ms = umax4(f[t]) == SENT; f[t] = ms ? n[t] : f[t]; }
        }
        float y[4], gt[4];
#pragma unroll
        for (int e = 0; e < 4; ++e) {
            float v = (bitsf(f[0][e]) + bitsf(f[1][e])) + (bitsf(f[2][e]) + bitsf(f[3][e]));
            v += dpp<ROR2>(v); v += dpp<ROR4>(v); v += dpp<ROR8>(v); y[e] = rbf(xrow_sum(v));
            const float sg = rbf(y[e] / (1.0f + __expf(-y[e])));
            gt[e] = dpp<ROR1>(sg);
        }
        if (lane == 0) {
            const float xv[4] = {bflo(xo.x), bfhi(xo.x), bflo(xo.y), bfhi(xo.y)};
            float o[4];
#pragma unroll
            for (int e = 0; e < 4; ++e) o[e] = xv[e] + rbf(y[e] * gt[e]);
            *(GAS v2u*)((unsigned char*)p.out + ocol * 2) = v2u{pk2(o[0], o[1]), pk2(o[2], o[3])};
            if (b == 0) __hip_atomic_store((gu64*)p.ws, E + 1, __ATOMIC_RELAXED, __HIP_MEMORY_SCOPE_AGENT);
        }
    }
    STAMP(12);
}
}

extern "C" void kernel_launch(void* const* d_in, const int* in_sizes, int n_in, void* d_out, int out_size, void* d_ws, size_t ws_size, hipStream_t stream) {
    (void)in_sizes; (void)n_in; (void)out_size; (void)ws_size;
    Params p;
    p.x = d_in[0]; p.kc = d_in[1]; p.vc = d_in[2]; p.wq = d_in[3]; p.wo = d_in[4];
    p.wg = d_in[6]; p.wu = d_in[7]; p.wd = d_in[8];
    p.out = d_out; p.ws = d_ws;
    (void)hipFuncSetAttribute((const void*)fused_decode, hipFuncAttributeMaxDynamicSharedMemorySize, LDS_BYTES);
    hipLaunchKernelGGL(fused_decode, dim3(NCU), dim3(NTHR), LDS_BYTES, stream, p);
}
```
